```python
import torch

def _softmax(x, axis=-1):
    return torch.softmax(x, dim=axis)
def _gelu(x):
    return torch.nn.functional.gelu(x, approximate="tanh")
def _silu(x):
    return torch.nn.functional.silu(x)
def _sigmoid(x):
    return torch.sigmoid(x)
def _layernorm(x, axis=-1, eps=1e-5):
    m = x.mean(dim=axis, keepdim=True)
    v = x.var(dim=axis, keepdim=True, unbiased=False)
    return (x - m) / torch.sqrt(v + eps)
def _scatter_add(tgt, idx, src, axis=0):
    return tgt.clone().index_add_(axis, idx, src)
def _segment_sum(src, seg, n):
    out = torch.zeros((n,) + src.shape[1:], dtype=src.dtype)
    return out.index_add_(0, seg, src)
def _pad(x, pw):
    flat = tuple(v for pair in reversed(pw) for v in pair)
    return torch.nn.functional.pad(x, flat)

kernel_name = 'sequential_preLN__ssm_conv_s4__mixer_s_mlp__prompt'

def setup_inputs():

    _w = (lambda a: (lambda t: t.to(torch.bfloat16) if t.is_floating_point() else t)(torch.as_tensor(a)))
    def _A_setup():
        B, L, D, N = 1, 2048, 512, 128
        A_re = -(lambda _x: torch.exp(torch.as_tensor(_x)))((lambda *s: torch.randn(*s, dtype=torch.bfloat16) * 3.2)(D, N))
        A_im = torch.pi * (lambda *s: torch.randn(*s, dtype=torch.bfloat16) * 0.05)(D, N)
        B_re = (lambda *s: torch.randn(*s, dtype=torch.bfloat16) * 0.1)(D, N)
        B_im = (lambda *s: torch.randn(*s, dtype=torch.bfloat16) * 0.1)(D, N)
        C_re = (lambda *s: torch.randn(*s, dtype=torch.bfloat16) * 0.1)(D, N)
        C_im = (lambda *s: torch.randn(*s, dtype=torch.bfloat16) * 0.1)(D, N)
        log_dt = (lambda *s: torch.randn(*s, dtype=torch.bfloat16) * 0.05)(D) * 0.5 - 4.0
        Dskip = (lambda *s: torch.randn(*s, dtype=torch.bfloat16) * 0.05)(D)
        return {
            "u": _w((lambda *s: torch.randn(*s, dtype=torch.bfloat16) * 0.05)(B, L, D)),
            "A_re": _w(A_re),
            "A_im": _w(A_im),
            "B_re": _w(B_re),
            "B_im": _w(B_im),
            "C_re": _w(C_re),
            "C_im": _w(C_im),
            "log_dt": _w(log_dt),
            "Dskip": _w(Dskip),
        }
    _A_in = _A_setup()

    def _B_setup():
        B, S, D, F = 1, 2048, 512, 1792
        return {
            "x":  _w((lambda *s: torch.randn(*s, dtype=torch.bfloat16) * 0.05)(B, S, D)),
            "w1": _w((lambda *s: torch.randn(*s, dtype=torch.bfloat16) * 3.2)(D, D) * (D ** -0.5)),
            "w2": _w((lambda *s: torch.randn(*s, dtype=torch.bfloat16) * 3.2)(D, D) * (D ** -0.5)),
            "w3": _w((lambda *s: torch.randn(*s, dtype=torch.bfloat16) * 3.2)(D, F) * (D ** -0.5)),
            "w4": _w((lambda *s: torch.randn(*s, dtype=torch.bfloat16) * 1.6)(F, D) * (F ** -0.5)),
        }
    _B_in = _B_setup()

    return {
        "A_u": _A_in["u"],
        "A_A_re": _A_in["A_re"],
        "A_A_im": _A_in["A_im"],
        "A_B_re": _A_in["B_re"],
        "A_B_im": _A_in["B_im"],
        "A_C_re": _A_in["C_re"],
        "A_C_im": _A_in["C_im"],
        "A_log_dt": _A_in["log_dt"],
        "A_Dskip": _A_in["Dskip"],
        "B_x": _B_in["x"],
        "B_w1": _B_in["w1"],
        "B_w2": _B_in["w2"],
        "B_w3": _B_in["w3"],
        "B_w4": _B_in["w4"],
    }

def reference(A_u, A_A_re, A_A_im, A_B_re, A_B_im, A_C_re, A_C_im, A_log_dt, A_Dskip, B_x, B_w1, B_w2, B_w3, B_w4):
    def _A_ref(u, A_re, A_im, B_re, B_im, C_re, C_im, log_dt, Dskip):
        Bsz, L, D = u.shape
        dt = torch.exp(log_dt)[:, None]
        A = A_re + 1j * A_im
        Bc = B_re + 1j * B_im
        Cc = C_re + 1j * C_im
        logdA = dt * A
        dB = dt * Bc
        l = (lambda x, dt: x.to(dt))(torch.arange(L), A_re.dtype)
        P = torch.exp(logdA[:, :, None] * l[None, None, :])
        K = torch.einsum("dn,dnl,dn->dl", Cc, P, dB).real
        L2 = 2 * L
        Kp = _pad(K, ((0, 0), (0, L)))
        up = (lambda x, *d: x.permute(*d))(u, 0, 2, 1)
        up = _pad(up, ((0, 0), (0, 0), (0, L)))
        Kf = (lambda x, axes=(-2,-1): torch.fft.rfft2(x.float(), dim=axes))(Kp[:, None, :], axes=(-2, -1))
        Uf = (lambda x, axes=(-2,-1): torch.fft.rfft2(x.float(), dim=axes))(up[:, :, None, :], axes=(-2, -1))
        Yf = Uf * Kf[None, :, :, :]
        y = (lambda x, s, axes=(-2,-1): torch.fft.irfft2(x, s=s, dim=axes).to(torch.bfloat16))(Yf, (1, L2), axes=(-2, -1))[:, :, 0, :L]
        y = (lambda x, *d: x.permute(*d))(y, 0, 2, 1) + u * Dskip[None, None, :]
        return y

    def _B_ref(x, w1, w2, w3, w4):
        B, S, D = x.shape
        h = _gelu(_layernorm(x, axis=-1) @ w1)
        c = D // 4
        g0 = _pad(h[:, :, 0*c:1*c], ((0, 0), (1, 0), (0, 0)))[:, :S, :]
        g1 = _pad(h[:, :, 1*c:2*c], ((0, 0), (0, 1), (0, 0)))[:, 1:, :]
        g2 = _pad(h[:, :, 2*c:3*c], ((0, 0), (2, 0), (0, 0)))[:, :S, :]
        g3 = _pad(h[:, :, 3*c:   ], ((0, 0), (0, 2), (0, 0)))[:, 2:, :]
        hs = torch.concatenate([g0, g1, g2, g3], axis=-1) @ w2
        x1 = x + hs
        h2 = _gelu(_layernorm(x1, axis=-1) @ w3) @ w4
        return x1 + h2

    x = A_u
    h = x + _A_ref(_layernorm(x, axis=-1), A_A_re, A_A_im, A_B_re, A_B_im, A_C_re, A_C_im, A_log_dt, A_Dskip)
    h2 = h + _B_ref(_layernorm(h, axis=-1), B_w1, B_w2, B_w3, B_w4)
    return h2
```

```cpp
#include <hip/hip_runtime.h>
#include <hip/hip_bf16.h>
#include <stdint.h>

#define DEV __device__ __forceinline__
typedef unsigned int u32;
typedef unsigned short u16;
typedef unsigned long long u64;
typedef float f32x2 __attribute__((ext_vector_type(2)));
typedef float f32x4 __attribute__((ext_vector_type(4)));
typedef float f32x16 __attribute__((ext_vector_type(16)));
typedef u32 u32x2 __attribute__((ext_vector_type(2)));
typedef u32 u32x4 __attribute__((ext_vector_type(4)));
typedef __bf16 bf16x2 __attribute__((ext_vector_type(2)));
typedef __bf16 bf16x8 __attribute__((ext_vector_type(8)));
typedef _Float16 f16x2 __attribute__((ext_vector_type(2)));
typedef _Float16 f16x8 __attribute__((ext_vector_type(8)));

constexpr int L = 2048, D = 512, NS = 128, F = 1792;

constexpr size_t WS_WT1 = 0;
constexpr size_t WS_WT2 = WS_WT1 + 512 * 512 * 2;
constexpr size_t WS_WT3 = WS_WT2 + 512 * 512 * 2;
constexpr size_t WS_WT4 = WS_WT3 + 512 * 1792 * 2;
constexpr size_t WS_CS1 = WS_WT4 + 512 * 1792 * 2;
constexpr size_t WS_CS3 = WS_CS1 + 512 * 4;
constexpr size_t WS_STAT = WS_CS3 + 1792 * 4;
constexpr size_t WS_H0 = WS_STAT + 2048 * 8;
constexpr size_t WS_XP = WS_H0 + 2048 * 512 * 2;
constexpr size_t WS_HG = WS_XP + 2048 * 512 * 2;
constexpr size_t WS_X1 = WS_HG + 2048 * 512 * 2;
constexpr size_t WS_PST = WS_X1 + 2048 * 512 * 2;
constexpr size_t WS_G = WS_PST + 2048 * 16 * 8;
constexpr size_t WS_END = WS_G + 2048 * 1792 * 2;

__device__ const u32x4 g_zero16[4] = {};

DEV float bf_lo(u32 v) { return __uint_as_float(v << 16); }
DEV float bf_hi(u32 v) { return __uint_as_float(v & 0xffff0000u); }
DEV float bf1(u16 v) { return __uint_as_float(((u32)v) << 16); }
DEV u32 pk_bf16(float a, float b) {
    f32x2 f = {a, b};
    bf16x2 r = __builtin_convertvector(f, bf16x2);
    return __builtin_bit_cast(u32, r);
}
DEV float rbf(float a) { return bf_lo(pk_bf16(a, 0.f)); }
DEV u32 pk_f16(float a, float b) {
    f32x2 f = {a, b};
    f16x2 r = __builtin_convertvector(f, f16x2);
    return __builtin_bit_cast(u32, r);
}
DEV float dot2bf(u32 a, u32 b, float c) {
    return __builtin_amdgcn_fdot2_f32_bf16(__builtin_bit_cast(bf16x2, a), __builtin_bit_cast(bf16x2, b), c, false);
}
DEV f32x16 mfma32(u32x4 a, u32x4 b, f32x16 c) {
    return __builtin_amdgcn_mfma_f32_32x32x16_bf16(__builtin_bit_cast(bf16x8, a), __builtin_bit_cast(bf16x8, b), c, 0, 0, 0);
}
DEV f32x4 mfma16bf(u32x4 a, u32x4 b, f32x4 c) {
    return __builtin_amdgcn_mfma_f32_16x16x32_bf16(__builtin_bit_cast(bf16x8, a), __builtin_bit_cast(bf16x8, b), c, 0, 0, 0);
}
DEV f32x4 mfma16h(u32x4 a, u32x4 b, f32x4 c) {
    return __builtin_amdgcn_mfma_f32_16x16x32_f16(__builtin_bit_cast(f16x8, a), __builtin_bit_cast(f16x8, b), c, 0, 0, 0);
}
DEV float gelu_f(float x) {
    const float c1 = -2.0f * 0.7978845608028654f * 1.4426950408889634f;
    const float c3 = c1 * 0.044715f;
    float x2 = x * x;
    float t = x * (c1 + c3 * x2);
    float e = __builtin_amdgcn_exp2f(t);
    return x * __builtin_amdgcn_rcpf(1.0f + e);
}
DEV float xor32(float v) { return __shfl_xor(v, 32); }
#define WAIT_V(n) asm volatile("s_waitcnt vmcnt(" #n ")" ::: "memory")
#define WAIT_L0() asm volatile("s_waitcnt lgkmcnt(0)" ::: "memory")
#define BAR() __builtin_amdgcn_s_barrier()

DEV void ln_consts(float mean, float var, float& m_bf, float& sd_bf) {
    m_bf = rbf(mean);
    float v = rbf(var);
    v = rbf(v + 1e-5f);
    sd_bf = rbf(__builtin_sqrtf(v));
}

__global__ __launch_bounds__(256) void k_prep(const u16* __restrict__ x, const u16* __restrict__ w1,
                                              const u16* __restrict__ w2, const u16* __restrict__ w3,
                                              const u16* __restrict__ w4, char* __restrict__ ws) {
    const int tid = threadIdx.x, lane = tid & 63, wave = tid >> 6;
    int b = blockIdx.x;
    if (b < 512) {
        int t = b * 4 + wave;
        u32x4 v = *(const u32x4*)(x + (size_t)t * D + lane * 8);
        float f[8];
        for (int i = 0; i < 4; ++i) { f[2 * i] = bf_lo(v[i]); f[2 * i + 1] = bf_hi(v[i]); }
        float s = 0;
        for (int i = 0; i < 8; ++i) s += f[i];
        for (int o = 1; o < 64; o <<= 1) s += __shfl_xor(s, o);
        float mean = s * (1.0f / D);
        float q = 0;
        for (int i = 0; i < 8; ++i) { float d = f[i] - mean; q += d * d; }
        for (int o = 1; o < 64; o <<= 1) q += __shfl_xor(q, o);
        float m, sd;
        ln_consts(mean, q * (1.0f / D), m, sd);
        if (lane == 0) ((u32*)(ws + WS_STAT))[t] = (__float_as_uint(m) >> 16) | (__float_as_uint(sd) & 0xffff0000u);
        return;
    }
    b -= 512;
    const u16* w; int N, nt, k0; u16* dst; float* cs = nullptr; int KS;
    if (b < 16) { w = w1; N = 512; nt = b; k0 = 0; dst = (u16*)(ws + WS_WT1); cs = (float*)(ws + WS_CS1); KS = 32; }
    else if (b < 32) { w = w2; N = 512; nt = b - 16; k0 = 0; dst = (u16*)(ws + WS_WT2); KS = 32; }
    else if (b < 88) { w = w3; N = 1792; nt = b - 32; k0 = 0; dst = (u16*)(ws + WS_WT3); cs = (float*)(ws + WS_CS3); KS = 32; }
    else { int u = b - 88; w = w4; N = 512; nt = u & 15; KS = 112; dst = (u16*)(ws + WS_WT4); k0 = (u >> 4) * 32;  }
    int nks = 32;
    if (b >= 88) { int q = (b - 88) >> 4; if (q == 3) nks = 16; }
    const int r = lane & 31, h = lane >> 5;
    float csum = 0;
    for (int s = wave; s < nks; s += 4) {
        int ks = k0 + s;
        u16 e[8];
#pragma unroll
        for (int j = 0; j < 8; ++j) {
            int k = 16 * ks + 8 * (j >> 2) + 4 * h + (j & 3);
            e[j] = w[(size_t)k * N + nt * 32 + r];
        }
        u32x4 o;
        for (int i = 0; i < 4; ++i) o[i] = (u32)e[2 * i] | ((u32)e[2 * i + 1] << 16);
        for (int j = 0; j < 8; ++j) csum += bf1(e[j]);
        *(u32x4*)(dst + (((size_t)nt * KS + ks) * 64 + lane) * 8) = o;
    }
    if (cs) {
        __shared__ float red[4][32];
        csum += xor32(csum);
        if (lane < 32) red[wave][lane] = csum;
        __syncthreads();
        if (tid < 32) cs[nt * 32 + tid] = red[0][tid] + red[1][tid] + red[2][tid] + red[3][tid];
    }
}

struct cplx { float r, i; };
DEV cplx cmul(cplx a, cplx b) { return {a.r * b.r - a.i * b.i, a.r * b.i + a.i * b.r}; }
DEV cplx cexp_pow2(float tr, float wh, float wl, float p2) {
    float mag = __builtin_amdgcn_exp2f(tr * p2);
    float ph = __builtin_amdgcn_fractf(wh * p2) + wl * p2;
    return {mag * __builtin_amdgcn_cosf(ph), mag * __builtin_amdgcn_sinf(ph)};
}
constexpr int KA_X = 0;
constexpr int KA_Y = KA_X + 2 * 48 * 1024;
constexpr int KA_U = KA_Y + 2 * 16 * 1024;
constexpr int UPAD = 240, ULEN = 2304;
constexpr int KA_KC = KA_U + 2 * ULEN * 2;
constexpr int KA_MX = KA_KC + 2 * 648 * 4;
constexpr int KA_LDS = KA_MX + 64;
constexpr int RS_STRIDE = 4160 + 64;
constexpr int RCOPY = 4160;
constexpr int KA_R = 0;
constexpr int KA_YP = 40960;

DEV void store_hl(char* base, int row, int n, cplx v, bool negim) {
    float re = v.r, im = negim ? -v.i : v.i;
    u32 hi = pk_bf16(re, im);
    u32 lo = pk_bf16(re - bf_lo(hi), im - bf_hi(hi));
    u32x2 o = {hi, lo};
    *(u32x2*)(base + row * 1024 + (((n >> 1) ^ (row & 15)) << 4) + ((n & 1) << 3)) = o;
}
DEV int lag_ci(int Lg) {
    u32 bits = __float_as_uint((float)Lg);
    u32 rb = (bits + 0x7FFFu + ((bits >> 16) & 1u)) >> 16;
    return Lg < 128 ? Lg : (int)rb - 0x4300 + 128;
}

__global__ __launch_bounds__(512) void k_conv(const u16* __restrict__ x, const u16* __restrict__ A_re,
                                              const u16* __restrict__ A_im, const u16* __restrict__ B_re,
                                              const u16* __restrict__ B_im, const u16* __restrict__ C_re,
                                              const u16* __restrict__ C_im, const u16* __restrict__ log_dt,
                                              const u16* __restrict__ Dskip, char* __restrict__ ws) {
    extern __shared__ __attribute__((aligned(16))) char lds[];
    const int tid = threadIdx.x, lane = tid & 63, wave = __builtin_amdgcn_readfirstlane(tid >> 6);
    const int b = blockIdx.x;
    const int cb = (b & 7) * 32 + (b >> 3);
    const int d0 = cb * 2;

    u32 xv[4];
#pragma unroll
    for (int k = 0; k < 4; ++k) xv[k] = *(const u32*)(x + (size_t)(tid * 4 + k) * D + d0);
    u32x4 st = *(const u32x4*)(ws + WS_STAT + tid * 16);

    {
        const int ch = wave >> 2, nh = wave & 1, part = (wave >> 1) & 1;
        const int d = d0 + ch, n = nh * 64 + lane;
        const int idx = d * NS + n;
        float are = bf1(A_re[idx]), aim = bf1(A_im[idx]);
        float bre = bf1(B_re[idx]), bim = bf1(B_im[idx]);
        float cre = bf1(C_re[idx]), cim = bf1(C_im[idx]);
        float dt = rbf(__expf(bf1(log_dt[d])));
        float zr = dt * are, zi = dt * aim;
        float tr = zr * 1.4426950408889634f;
        const float inv2pi_h = 0.15915494309189535f;
        const float inv2pi_l = (float)(0.15915494309189533576888 - (double)inv2pi_h);
        float wh = zi * inv2pi_h;
        float wl = __builtin_fmaf(zi, inv2pi_h, -wh) + zi * inv2pi_l;
        cplx dB = {dt * bre, dt * bim};
        cplx CB = cmul({cre, cim}, dB);
        char* Xb = lds + KA_X + ch * 48 * 1024;
        char* Yb = lds + KA_Y + ch * 16 * 1024;
        if (part == 0) {
            cplx P1 = cexp_pow2(tr, wh, wl, 1.f), P2 = cexp_pow2(tr, wh, wl, 2.f), P4 = cexp_pow2(tr, wh, wl, 4.f);
            cplx P8 = cexp_pow2(tr, wh, wl, 8.f), P16 = cexp_pow2(tr, wh, wl, 16.f), P32 = cexp_pow2(tr, wh, wl, 32.f);
            cplx P64 = cexp_pow2(tr, wh, wl, 64.f), P128 = cexp_pow2(tr, wh, wl, 128.f);
            cplx Y[16];
            Y[0] = {1.f, 0.f}; Y[1] = P8; Y[2] = P16; Y[3] = cmul(P8, P16); Y[4] = P32;
            Y[5] = cmul(P32, P8); Y[6] = cmul(P32, P16); Y[7] = cmul(P32, Y[3]); Y[8] = P64;
#pragma unroll
            for (int j = 1; j < 8; ++j) Y[8 + j] = cmul(P64, Y[j]);
#pragma unroll
            for (int j = 0; j < 16; ++j) store_hl(Yb, j, n, Y[j], false);
            cplx Xa[8];
            Xa[0] = CB; Xa[1] = cmul(CB, P1); Xa[2] = cmul(CB, P2); Xa[3] = cmul(Xa[2], P1);
#pragma unroll
            for (int j = 0; j < 4; ++j) Xa[4 + j] = cmul(Xa[j], P4);
#pragma unroll
            for (int j = 0; j < 8; ++j) { store_hl(Xb, j, n, Xa[j], true); store_hl(Xb, 8 + j, n, cmul(Xa[j], P128), true); }
        } else {
            cplx P2 = cexp_pow2(tr, wh, wl, 2.f), P4 = cexp_pow2(tr, wh, wl, 4.f), P128 = cexp_pow2(tr, wh, wl, 128.f);
            cplx P256 = cexp_pow2(tr, wh, wl, 256.f), P512 = cexp_pow2(tr, wh, wl, 512.f);
            cplx P1024 = cexp_pow2(tr, wh, wl, 1024.f), P2048 = cexp_pow2(tr, wh, wl, 2048.f);
            cplx X0 = CB, X2 = cmul(CB, P2), X4 = cmul(CB, P4), X6 = cmul(X2, P4);
            cplx X128 = cmul(CB, P128), X132 = cmul(X4, P128);
            cplx c0[4] = {cmul(X0, P256), cmul(X2, P256), cmul(X4, P256), cmul(X6, P256)};
            cplx c1[4];
#pragma unroll
            for (int j = 0; j < 4; ++j) c1[j] = cmul(c0[j], P128);
#pragma unroll
            for (int j = 0; j < 4; ++j) { store_hl(Xb, 16 + j, n, c0[j], true); store_hl(Xb, 20 + j, n, c1[j], true); }
            cplx dd[8] = {cmul(X0, P512), cmul(X4, P512), cmul(X128, P512), cmul(X132, P512),
                          cmul(c0[0], P512), cmul(c0[2], P512), cmul(c1[0], P512), cmul(c1[2], P512)};
#pragma unroll
            for (int j = 0; j < 8; ++j) store_hl(Xb, 24 + j, n, dd[j], true);
            cplx ee[8] = {X0, X128, c0[0], c1[0], dd[0], dd[2], dd[4], dd[6]};
#pragma unroll
            for (int j = 0; j < 8; ++j) store_hl(Xb, 32 + j, n, cmul(ee[j], P1024), true);
            store_hl(Xb, 40, n, cmul(CB, P2048), true);
        }
    }
    float ud[2][4];
    {
        float ds0 = bf1(Dskip[d0]), ds1 = bf1(Dskip[d0 + 1]);
        float u0[4], u1[4];
#pragma unroll
        for (int k = 0; k < 4; ++k) {
            float m = bf_lo(st[k]), sd = bf_hi(st[k]);
            u0[k] = rbf(rbf(bf_lo(xv[k]) - m) / sd);
            u1[k] = rbf(rbf(bf_hi(xv[k]) - m) / sd);
            ud[0][k] = rbf(u0[k] * ds0);
            ud[1][k] = rbf(u1[k] * ds1);
        }
        u32x2 a = {pk_f16(u0[0], u0[1]), pk_f16(u0[2], u0[3])};
        u32x2 c = {pk_f16(u1[0], u1[1]), pk_f16(u1[2], u1[3])};
        *(u32x2*)(lds + KA_U + (UPAD + tid * 4) * 2) = a;
        *(u32x2*)(lds + KA_U + ULEN * 2 + (UPAD + tid * 4) * 2) = c;
        if (tid < 60) {
            u32x2 z = {0, 0};
            *(u32x2*)(lds + KA_U + tid * 8) = z;
            *(u32x2*)(lds + KA_U + ULEN * 2 + tid * 8) = z;
        }
    }
    __syncthreads();
    {
        const int ch = wave >> 2, mt = wave & 3;
        if (mt < 3) {
            const char* Xb = lds + KA_X + ch * 48 * 1024;
            const char* Yb = lds + KA_Y + ch * 16 * 1024;
            const int r = lane & 15, g = lane >> 4;
            f32x4 acc0 = {0, 0, 0, 0}, acc1 = {0, 0, 0, 0};
#pragma unroll
            for (int ks = 0; ks < 16; ++ks) {
                int sw = ((ks * 4 + g) ^ r) << 4;
                u32x4 a = *(const u32x4*)(Xb + (mt * 16 + r) * 1024 + sw);
                u32x4 y = *(const u32x4*)(Yb + r * 1024 + sw);
                u32x4 b1 = {y[0], y[0], y[2], y[2]}, b2 = {y[1], y[1], y[3], y[3]};
                acc0 = mfma16bf(a, b1, acc0);
                acc1 = mfma16bf(a, b2, acc1);
            }
            float* Kc = (float*)(lds + KA_KC) + ch * 648;
            float mx = 0.f;
#pragma unroll
            for (int e = 0; e < 4; ++e) {
                int row = mt * 16 + g * 4 + e, j = r;
                float kv = acc0[e] + acc1[e];
                int lrow;
                if (row < 16) lrow = (row & 7) + 128 * (row >> 3);
                else if (row < 24) lrow = 256 + 2 * (row & 3) + 128 * ((row >> 2) & 1);
                else if (row < 32) lrow = 512 + 4 * (row & 1) + 128 * ((row >> 1) & 3);
                else lrow = 1024 + 128 * (row & 7) + (row >= 40 ? 1024 : 0);
                bool valid = row < 40 || (row == 40 && j == 0);
                if (valid) {
                    Kc[lag_ci(lrow + 8 * j)] = kv;
                    mx = fmaxf(mx, fabsf(kv));
                }
            }
            for (int o = 1; o < 64; o <<= 1) mx = fmaxf(mx, __shfl_xor(mx, o));
            if (lane == 0) ((float*)(lds + KA_MX))[wave] = mx;
        }
    }
    __syncthreads();
    float inv_scale[2];
    {
        const float* mxp = (const float*)(lds + KA_MX);
        float sc[2];
#pragma unroll
        for (int ch = 0; ch < 2; ++ch) {
            float mx = fmaxf(fmaxf(mxp[ch * 4], mxp[ch * 4 + 1]), mxp[ch * 4 + 2]);
            int e = (int)((__float_as_uint(mx) >> 23) & 0xff);
            e = e < 16 ? 16 : (e > 240 ? 240 : e);
            sc[ch] = __uint_as_float((u32)(127 + 14 + 127 - e) << 23);
            inv_scale[ch] = __uint_as_float((u32)(e - 14) << 23);
        }
        for (int task = tid; task < 2 * 260; task += 512) {
            int ch = task >= 260 ? 1 : 0;
            int c = task - ch * 260;
            const float* Kc = (const float*)(lds + KA_KC) + ch * 648;
            float v[12];
#pragma unroll
            for (int i = 0; i < 12; ++i) {
                int lag = 2047 - (8 * c + i);
                float kv = Kc[lag_ci(lag < 0 ? 0 : lag)];
                v[i] = lag < 0 ? 0.f : kv * sc[ch];
            }
            u32 pe[6], po[5];
#pragma unroll
            for (int i = 0; i < 6; ++i) pe[i] = pk_f16(v[2 * i], v[2 * i + 1]);
#pragma unroll
            for (int i = 0; i < 5; ++i) po[i] = __builtin_amdgcn_alignbit(pe[i + 1], pe[i], 16);
            char* Rb = lds + KA_R + ch * 4 * RCOPY + c * 16;
            *(u32x4*)(Rb + 0 * RCOPY) = u32x4{pe[0], pe[1], pe[2], pe[3]};
            *(u32x4*)(Rb + 1 * RCOPY) = u32x4{po[0], po[1], po[2], po[3]};
            *(u32x4*)(Rb + 2 * RCOPY) = u32x4{pe[1], pe[2], pe[3], pe[4]};
            *(u32x4*)(Rb + 3 * RCOPY) = u32x4{po[1], po[2], po[3], po[4]};
        }
    }
    __syncthreads();
    {
        const int ch = wave >> 2, rh = (wave >> 1) & 1, cg = wave & 1;
        const int r = lane & 15, g = lane >> 4;
        const int o0 = 15 - r + 8 * g;
        const char* Ab = lds + KA_R + ch * 4 * RCOPY + (o0 & 3) * RCOPY + (o0 & ~3) * 2;
        const char* Bb = lds + KA_U + ch * ULEN * 2 + (16 * r + 8 * g) * 2;
        f32x4 acc[4];
#pragma unroll
        for (int i = 0; i < 4; ++i) acc[i] = f32x4{0, 0, 0, 0};
#pragma unroll
        for (int rr = 0; rr < 4; ++rr) {
            const int rho = rh * 4 + rr;
            const char* Ar = Ab + (7 - rho) * 64;
            const char* Br = Bb + (7 - rho) * 64;
            if (cg == 0) {
                u32x4 Af[8], Bf[8];
#pragma unroll
                for (int a = 0; a < 8; ++a) {
                    u32x2 lo = *(const u32x2*)(Ar + (7 - a) * 512);
                    u32x2 hi = *(const u32x2*)(Ar + (7 - a) * 512 + 8);
                    Af[a] = u32x4{lo[0], lo[1], hi[0], hi[1]};
                    Bf[a] = *(const u32x4*)(Br + a * 512);
                }
                acc[0] = mfma16h(Af[0], Bf[0], acc[0]);
#pragma unroll
                for (int a = 0; a <= 1; ++a) acc[1] = mfma16h(Af[a], Bf[1 - a], acc[1]);
#pragma unroll
                for (int a = 0; a <= 6; ++a) acc[2] = mfma16h(Af[a], Bf[6 - a], acc[2]);
#pragma unroll
                for (int a = 0; a <= 7; ++a) acc[3] = mfma16h(Af[a], Bf[7 - a], acc[3]);
            } else {
                u32x4 Af[6], Bf[6];
#pragma unroll
                for (int a = 0; a < 6; ++a) {
                    u32x2 lo = *(const u32x2*)(Ar + (7 - a) * 512);
                    u32x2 hi = *(const u32x2*)(Ar + (7 - a) * 512 + 8);
                    Af[a] = u32x4{lo[0], lo[1], hi[0], hi[1]};
                    Bf[a] = *(const u32x4*)(Br + a * 512);
                }
#pragma unroll
                for (int c = 2; c <= 5; ++c)
#pragma unroll
                    for (int a = 0; a <= c; ++a) acc[c - 2] = mfma16h(Af[a], Bf[c - a], acc[c - 2]);
            }
        }
        float* Yp = (float*)(lds + KA_YP) + (rh * 2 + ch) * 2048;
#pragma unroll
        for (int i = 0; i < 4; ++i) {
            int c = cg == 0 ? (i < 2 ? i : i + 4) : i + 2;
            *(f32x4*)(Yp + 256 * c + 16 * r + 4 * g) = acc[i];
        }
    }
    __syncthreads();
    {
        const float* Yp = (const float*)(lds + KA_YP);
        f32x4 y0 = *(const f32x4*)(Yp + 0 * 2048 + tid * 4) + *(const f32x4*)(Yp + 2 * 2048 + tid * 4);
        f32x4 y1 = *(const f32x4*)(Yp + 1 * 2048 + tid * 4) + *(const f32x4*)(Yp + 3 * 2048 + tid * 4);
        const int s = d0 >> 4, cc = d0 & 15, h = (cc >> 2) & 1, j = (cc & 3) + 4 * (cc >> 3);
        u16* hT = (u16*)(ws + WS_H0);
#pragma unroll
        for (int k = 0; k < 4; ++k) {
            int t = tid * 4 + k;
            float a0 = rbf(rbf(y0[k] * inv_scale[0]) + ud[0][k]);
            float a1 = rbf(rbf(y1[k] * inv_scale[1]) + ud[1][k]);
            u32 o = pk_bf16(bf_lo(xv[k]) + a0, bf_hi(xv[k]) + a1);
            size_t off = ((((size_t)(t >> 5) * 32 + s) * 64) + h * 32 + (t & 31)) * 8 + j;
            *(u32*)(hT + off) = o;
        }
    }
}

constexpr int G_ACT = 0, G_W = 65536, G_RED = 131072, G_PS = G_RED + 16384;
constexpr int G12_LDS = G_PS + 8192;

DEV void glds16(const void* g, char* l) {
    __builtin_amdgcn_global_load_lds((const __attribute__((address_space(1))) void*)g,
                                     (__attribute__((address_space(3))) void*)l, 16, 0, 0);
}

template <int WHICH>
__global__ __launch_bounds__(512) void k_gemm12(char* __restrict__ ws) {
    extern __shared__ __attribute__((aligned(16))) char lds[];
    const int tid = threadIdx.x, lane = tid & 63, wave = __builtin_amdgcn_readfirstlane(tid >> 6);
    const int b = blockIdx.x;
    const int q = b >> 3;
    const int tt = (b & 7) * 4 + (q >> 3), ct = q & 7;
    const int r = lane & 31, h = lane >> 5;
    const char* act = ws + (WHICH == 1 ? WS_H0 : WS_HG);
    const char* wt = ws + (WHICH == 1 ? WS_WT1 : WS_WT2);
#pragma unroll
    for (int c = 0; c < 4; ++c) {
        const int ks = 8 * c + wave;
#pragma unroll
        for (int i = 0; i < 2; ++i) {
            const char* src;
            if (WHICH == 1) {
                src = act + ((((size_t)(tt * 2 + i) * 32 + ks) * 64) + lane) * 16;
            } else {
                const int sh = c == 0 ? -1 : (c == 1 ? 1 : (c == 2 ? -2 : 2));
                int t = tt * 64 + i * 32 + r + sh;
                bool ok = t >= 0 && t < L;
                src = ok ? act + ((((size_t)(t >> 5) * 32 + ks) * 64) + h * 32 + (t & 31)) * 16 : (const char*)g_zero16;
            }
            glds16(src, lds + G_ACT + (i * 32 + ks) * 1024);
        }
#pragma unroll
        for (int i = 0; i < 2; ++i)
            glds16(wt + ((((size_t)(ct * 2 + i) * 32 + ks) * 64) + lane) * 16, lds + G_W + (i * 32 + ks) * 1024);
    }
    const int tq = wave & 1, nq = (wave >> 1) & 1, kh = wave >> 2;
    f32x16 acc = {};
    u32x4 own[4][2];
    float s1[2] = {0, 0}, q1[2] = {0, 0};
    const u32 ones = 0x3f803f80u;
#pragma unroll
    for (int c = 0; c < 4; ++c) {
        if (c == 0) WAIT_V(12); else if (c == 1) WAIT_V(8); else if (c == 2) WAIT_V(4); else WAIT_V(0);
        BAR();
#pragma unroll
        for (int kk = 0; kk < 4; ++kk) {
            const int ks = 8 * c + 4 * kh + kk;
            u32x4 a = *(const u32x4*)(lds + G_W + (nq * 32 + ks) * 1024 + lane * 16);
            u32x4 bb = *(const u32x4*)(lds + G_ACT + (tq * 32 + ks) * 1024 + lane * 16);
            acc = mfma32(a, bb, acc);
        }
        if (WHICH == 1) {
#pragma unroll
            for (int i = 0; i < 2; ++i) {
                own[c][i] = *(const u32x4*)(lds + G_ACT + (i * 32 + 8 * c + wave) * 1024 + lane * 16);
#pragma unroll
                for (int e = 0; e < 4; ++e) {
                    s1[i] = dot2bf(own[c][i][e], ones, s1[i]);
                }
            }
        }
    }
    if (kh == 1) {
#pragma unroll
        for (int i = 0; i < 4; ++i)
            *(f32x4*)(lds + G_RED + ((wave & 3) * 4 + i) * 1024 + lane * 16) =
                f32x4{acc[4 * i], acc[4 * i + 1], acc[4 * i + 2], acc[4 * i + 3]};
    }
    float alpha = 1.f, beta = 0.f;
    if (WHICH == 1) {
        f32x2* ps = (f32x2*)(lds + G_PS);
#pragma unroll
        for (int i = 0; i < 2; ++i) {
            float st = s1[i] + xor32(s1[i]);
            float mu = st * (1.0f / 64);
            float qq = 0;
#pragma unroll
            for (int c = 0; c < 4; ++c)
#pragma unroll
                for (int e = 0; e < 4; ++e) {
                    float a = bf_lo(own[c][i][e]) - mu, bq = bf_hi(own[c][i][e]) - mu;
                    qq += a * a + bq * bq;
                }
            qq += xor32(qq);
            if (h == 0) ps[(i * 32 + r) * 8 + wave] = f32x2{st, qq};
        }
        __syncthreads();
        float m1[2], sd1[2];
#pragma unroll
        for (int i = 0; i < 2; ++i) {
            f32x4 p[4];
#pragma unroll
            for (int e = 0; e < 4; ++e) p[e] = *(const f32x4*)(ps + (i * 32 + r) * 8 + 2 * e);
            float st = 0;
#pragma unroll
            for (int e = 0; e < 4; ++e) st += p[e][0] + p[e][2];
            float mean = st * (1.0f / 512);
            float M2 = 0;
#pragma unroll
            for (int e = 0; e < 4; ++e) {
                float da = p[e][0] * (1.0f / 64) - mean, db = p[e][2] * (1.0f / 64) - mean;
                M2 += p[e][1] + p[e][3] + 64.f * (da * da + db * db);
            }
            ln_consts(mean, M2 * (1.0f / 512), m1[i], sd1[i]);
        }
        f32x2* ps2 = ps + 512;
#pragma unroll
        for (int i = 0; i < 2; ++i) {
            float rs = 1.0f / sd1[i];
            float st = 0;
#pragma unroll
            for (int c = 0; c < 4; ++c)
#pragma unroll
                for (int e = 0; e < 4; ++e) {
                    u32 dpk = pk_bf16(bf_lo(own[c][i][e]) - m1[i], bf_hi(own[c][i][e]) - m1[i]);
                    u32 o = pk_bf16(bf_lo(dpk) * rs, bf_hi(dpk) * rs);
                    own[c][i][e] = o;
                    st = dot2bf(o, ones, st);
                }
            st += xor32(st);
            float mu = st * (1.0f / 64);
            float qq = 0;
#pragma unroll
            for (int c = 0; c < 4; ++c)
#pragma unroll
                for (int e = 0; e < 4; ++e) {
                    float a = bf_lo(own[c][i][e]) - mu, bq = bf_hi(own[c][i][e]) - mu;
                    qq += a * a + bq * bq;
                }
            qq += xor32(qq);
            if (h == 0) ps2[(i * 32 + r) * 8 + wave] = f32x2{st, qq};
        }
#pragma unroll
        for (int c = 0; c < 4; ++c) {
            const int ks = 8 * c + wave;
            if ((ks >> 2) == ct) {
#pragma unroll
                for (int i = 0; i < 2; ++i)
                    *(u32x4*)(ws + WS_XP + ((((size_t)(tt * 2 + i) * 32 + ks) * 64) + lane) * 16) = own[c][i];
            }
        }
        __syncthreads();
        {
            f32x4 p[4];
#pragma unroll
            for (int e = 0; e < 4; ++e) p[e] = *(const f32x4*)(ps2 + (tq * 32 + r) * 8 + 2 * e);
            float st = 0;
#pragma unroll
            for (int e = 0; e < 4; ++e) st += p[e][0] + p[e][2];
            float mean = st * (1.0f / 512);
            float M2 = 0;
#pragma unroll
            for (int e = 0; e < 4; ++e) {
                float da = p[e][0] * (1.0f / 64) - mean, db = p[e][2] * (1.0f / 64) - mean;
                M2 += p[e][1] + p[e][3] + 64.f * (da * da + db * db);
            }
            float m2, sd2;
            ln_consts(mean, M2 * (1.0f / 512), m2, sd2);
            float r1 = 1.0f / (tq ? sd1[1] : sd1[0]), r2 = 1.0f / sd2;
            float mm1 = tq ? m1[1] : m1[0];
            alpha = r1 * r2;
            beta = alpha * mm1 + r2 * m2;
        }
    } else {
        __syncthreads();
    }
    if (kh == 0) {
#pragma unroll
        for (int i = 0; i < 4; ++i) {
            f32x4 p = *(const f32x4*)(lds + G_RED + (wave * 4 + i) * 1024 + lane * 16);
#pragma unroll
            for (int e = 0; e < 4; ++e) acc[4 * i + e] += p[e];
        }
        const int n0 = ct * 64 + nq * 32;
        const size_t tile = tt * 2 + tq;
        if (WHICH == 1) {
            const float* cs = (const float*)(ws + WS_CS1) + n0 + 4 * h;
            float o[16];
#pragma unroll
            for (int i = 0; i < 4; ++i) {
                f32x4 c4 = *(const f32x4*)(cs + 8 * i);
#pragma unroll
                for (int e = 0; e < 4; ++e) o[4 * i + e] = gelu_f(rbf(alpha * acc[4 * i + e] - beta * c4[e]));
            }
#pragma unroll
            for (int s = 0; s < 2; ++s) {
                u32x4 v = {pk_bf16(o[8 * s], o[8 * s + 1]), pk_bf16(o[8 * s + 2], o[8 * s + 3]),
                           pk_bf16(o[8 * s + 4], o[8 * s + 5]), pk_bf16(o[8 * s + 6], o[8 * s + 7])};
                *(u32x4*)(ws + WS_HG + (((tile * 32 + (n0 >> 4) + s) * 64) + lane) * 16) = v;
            }
        } else {
            float o[16];
            float st = 0;
#pragma unroll
            for (int s = 0; s < 2; ++s) {
                size_t off = (((tile * 32 + (n0 >> 4) + s) * 64) + lane) * 16;
                u32x4 xp = *(const u32x4*)(ws + WS_XP + off);
                u32x4 v;
#pragma unroll
                for (int e = 0; e < 4; ++e) {
                    u32 hs = pk_bf16(acc[8 * s + 2 * e], acc[8 * s + 2 * e + 1]);
                    u32 x1 = pk_bf16(bf_lo(xp[e]) + bf_lo(hs), bf_hi(xp[e]) + bf_hi(hs));
                    v[e] = x1;
                    o[8 * s + 2 * e] = bf_lo(x1); o[8 * s + 2 * e + 1] = bf_hi(x1);
                    st += bf_lo(x1) + bf_hi(x1);
                }
                *(u32x4*)(ws + WS_X1 + off) = v;
            }
            st += xor32(st);
            float mu = st * (1.0f / 32);
            float qq = 0;
#pragma unroll
            for (int e = 0; e < 16; ++e) { float dd = o[e] - mu; qq += dd * dd; }
            qq += xor32(qq);
            if (h == 0) ((f32x2*)(ws + WS_PST))[(tile * 32 + r) * 16 + ct * 2 + nq] = f32x2{st, qq};
        }
    }
}

constexpr int G3_LDS = 65536 + 64 * 8;
__global__ __launch_bounds__(512) void k_gemm3(char* __restrict__ ws) {
    extern __shared__ __attribute__((aligned(16))) char lds[];
    const int tid = threadIdx.x, lane = tid & 63, wave = __builtin_amdgcn_readfirstlane(tid >> 6);
    const int b = blockIdx.x;
    const int q = b >> 3;
    const int tt = (b & 7) * 4 + (q >> 3), ct = q & 7;
    const int r = lane & 31, h = lane >> 5;
    u32x4 av[8];
    const char* act = ws + WS_X1 + (size_t)tt * 65536;
#pragma unroll
    for (int i = 0; i < 8; ++i) av[i] = *(const u32x4*)(act + ((wave * 8 + i) * 64 + lane) * 16);
    if (wave == 7) {
        const f32x4* pp = (const f32x4*)(ws + WS_PST) + (size_t)(tt * 64 + lane) * 8;
        f32x4 p[8];
#pragma unroll
        for (int e = 0; e < 8; ++e) p[e] = pp[e];
        float st = 0;
#pragma unroll
        for (int e = 0; e < 8; ++e) st += p[e][0] + p[e][2];
        float mean = st * (1.0f / 512);
        float M2 = 0;
#pragma unroll
        for (int e = 0; e < 8; ++e) {
            float da = p[e][0] * (1.0f / 32) - mean, db = p[e][2] * (1.0f / 32) - mean;
            M2 += p[e][1] + p[e][3] + 32.f * (da * da + db * db);
        }
        float m, sd;
        ln_consts(mean, M2 * (1.0f / 512), m, sd);
        ((f32x2*)(lds + 65536))[lane] = f32x2{m, 1.0f / sd};
#pragma unroll
        for (int i = 0; i < 8; ++i) *(u32x4*)(lds + ((wave * 8 + i) * 64 + lane) * 16) = av[i];
        __syncthreads();
        return;
    }
    const int nt = ct * 7 + wave;
    u32x4 wf[32];
    const char* wt = ws + WS_WT3 + (size_t)nt * 32768 + lane * 16;
#pragma unroll
    for (int ks = 0; ks < 32; ++ks) wf[ks] = *(const u32x4*)(wt + ks * 1024);
    f32x4 c4[4];
    {
        const float* cs = (const float*)(ws + WS_CS3) + nt * 32 + 4 * h;
#pragma unroll
        for (int i = 0; i < 4; ++i) c4[i] = *(const f32x4*)(cs + 8 * i);
    }
#pragma unroll
    for (int i = 0; i < 8; ++i) *(u32x4*)(lds + ((wave * 8 + i) * 64 + lane) * 16) = av[i];
    __syncthreads();
    f32x16 acc0 = {}, acc1 = {};
#pragma unroll
    for (int ks = 0; ks < 32; ++ks) {
        u32x4 b0 = *(const u32x4*)(lds + ks * 1024 + lane * 16);
        u32x4 b1 = *(const u32x4*)(lds + (32 + ks) * 1024 + lane * 16);
        acc0 = mfma32(wf[ks], b0, acc0);
        acc1 = mfma32(wf[ks], b1, acc1);
    }
#pragma unroll
    for (int i = 0; i < 2; ++i) {
        f32x2 st = ((const f32x2*)(lds + 65536))[i * 32 + r];
        float o[16];
#pragma unroll
        for (int e = 0; e < 16; ++e) {
            float dv = i ? acc1[e] : acc0[e];
            o[e] = gelu_f(rbf((dv - st[0] * c4[e >> 2][e & 3]) * st[1]));
        }
        const size_t tile = tt * 2 + i;
#pragma unroll
        for (int s = 0; s < 2; ++s) {
            u32x4 v = {pk_bf16(o[8 * s], o[8 * s + 1]), pk_bf16(o[8 * s + 2], o[8 * s + 3]),
                       pk_bf16(o[8 * s + 4], o[8 * s + 5]), pk_bf16(o[8 * s + 6], o[8 * s + 7])};
            *(u32x4*)(ws + WS_G + (((tile * 112 + nt * 2 + s) * 64) + lane) * 16) = v;
        }
    }
}

constexpr int G4_LDS = 48 * 1024;
constexpr int G4_PF = 10;
__global__ __launch_bounds__(256, 1) void k_gemm4(char* __restrict__ ws, u16* __restrict__ out) {
    extern __shared__ __attribute__((aligned(16))) char lds[];
    const int tid = threadIdx.x, lane = tid & 63, wave = __builtin_amdgcn_readfirstlane(tid >> 6);
    const int b = blockIdx.x;
    const int q = b >> 3;
    const int tt = (b & 7) * 4 + (q >> 3), ct = q & 7;
    const int r = lane & 31, h = lane >> 5;
    const char* a0p = ws + WS_G + (((size_t)(tt * 2) * 112 + wave * 28) * 64 + lane) * 16;
    const char* a1p = a0p + 112 * 1024;
    const char* w0p = ws + WS_WT4 + (((size_t)(ct * 2) * 112 + wave * 28) * 64 + lane) * 16;
    const char* w1p = w0p + 112 * 1024;
    u32x4 fa0[G4_PF], fa1[G4_PF], fw0[G4_PF], fw1[G4_PF];
#pragma unroll
    for (int s = 0; s < G4_PF; ++s) {
        fw0[s] = *(const u32x4*)(w0p + s * 1024);
        fa0[s] = *(const u32x4*)(a0p + s * 1024);
        fw1[s] = *(const u32x4*)(w1p + s * 1024);
        fa1[s] = *(const u32x4*)(a1p + s * 1024);
    }
    const int tq = wave & 1, nq = wave >> 1;
    const size_t roff = ((((size_t)(tt * 2 + tq) * 32 + ct * 4 + nq * 2) * 64) + lane) * 16;
    u32x4 rx1[2], rh[2];
#pragma unroll
    for (int s = 0; s < 2; ++s) {
        rx1[s] = *(const u32x4*)(ws + WS_X1 + roff + s * 1024);
        rh[s] = *(const u32x4*)(ws + WS_H0 + roff + s * 1024);
    }
    f32x16 acc[4] = {};
#pragma unroll
    for (int s = 0; s < 28; ++s) {
        const int sl = s % G4_PF;
        acc[0] = mfma32(fw0[sl], fa0[sl], acc[0]);
        acc[1] = mfma32(fw0[sl], fa1[sl], acc[1]);
        acc[2] = mfma32(fw1[sl], fa0[sl], acc[2]);
        acc[3] = mfma32(fw1[sl], fa1[sl], acc[3]);
        if (s + G4_PF < 28) {
            fw0[sl] = *(const u32x4*)(w0p + (s + G4_PF) * 1024);
            fa0[sl] = *(const u32x4*)(a0p + (s + G4_PF) * 1024);
            fw1[sl] = *(const u32x4*)(w1p + (s + G4_PF) * 1024);
            fa1[sl] = *(const u32x4*)(a1p + (s + G4_PF) * 1024);
        }
    }
#pragma unroll
    for (int qd = 0; qd < 4; ++qd) {
        if (qd != wave) {
            int slot = wave > qd ? wave - 1 : wave;
#pragma unroll
            for (int i = 0; i < 4; ++i)
                *(f32x4*)(lds + ((qd * 3 + slot) * 4 + i) * 1024 + lane * 16) =
                    f32x4{acc[qd][4 * i], acc[qd][4 * i + 1], acc[qd][4 * i + 2], acc[qd][4 * i + 3]};
        }
    }
    __syncthreads();
    f32x16 fin = wave == 0 ? acc[0] : (wave == 1 ? acc[1] : (wave == 2 ? acc[2] : acc[3]));
#pragma unroll
    for (int sl = 0; sl < 3; ++sl)
#pragma unroll
        for (int i = 0; i < 4; ++i) {
            f32x4 p = *(const f32x4*)(lds + ((wave * 3 + sl) * 4 + i) * 1024 + lane * 16);
#pragma unroll
            for (int e = 0; e < 4; ++e) fin[4 * i + e] += p[e];
        }
    const int t = tt * 64 + tq * 32 + r;
    u16* orow = out + (size_t)t * D + ct * 64 + nq * 32 + 4 * h;
#pragma unroll
    for (int s = 0; s < 2; ++s) {
        u32 o[4];
#pragma unroll
        for (int e = 0; e < 4; ++e) {
            u32 h2 = pk_bf16(fin[8 * s + 2 * e], fin[8 * s + 2 * e + 1]);
            u32 t1 = pk_bf16(bf_lo(rx1[s][e]) + bf_lo(h2), bf_hi(rx1[s][e]) + bf_hi(h2));
            o[e] = pk_bf16(bf_lo(rh[s][e]) + bf_lo(t1), bf_hi(rh[s][e]) + bf_hi(t1));
        }
        *(u32x2*)(orow + 16 * s) = u32x2{o[0], o[1]};
        *(u32x2*)(orow + 16 * s + 8) = u32x2{o[2], o[3]};
    }
}

extern "C" void kernel_launch(void* const* d_in, const int* in_sizes, int n_in, void* d_out, int out_size,
                              void* d_ws, size_t ws_size, hipStream_t stream) {
    const u16* x = (const u16*)d_in[0];
    char* ws = (char*)d_ws;
    static bool init = false;
    if (!init) {
        hipFuncSetAttribute((const void*)k_conv, hipFuncAttributeMaxDynamicSharedMemorySize, KA_LDS);
        hipFuncSetAttribute((const void*)k_gemm12<1>, hipFuncAttributeMaxDynamicSharedMemorySize, G12_LDS);
        hipFuncSetAttribute((const void*)k_gemm12<2>, hipFuncAttributeMaxDynamicSharedMemorySize, G12_LDS);
        hipFuncSetAttribute((const void*)k_gemm3, hipFuncAttributeMaxDynamicSharedMemorySize, G3_LDS);
        init = true;
    }
    k_prep<<<512 + 32 + 56 + 64, 256, 0, stream>>>(x, (const u16*)d_in[10], (const u16*)d_in[11], (const u16*)d_in[12],
                                                   (const u16*)d_in[13], ws);
    k_conv<<<256, 512, KA_LDS, stream>>>(x, (const u16*)d_in[1], (const u16*)d_in[2], (const u16*)d_in[3],
                                         (const u16*)d_in[4], (const u16*)d_in[5], (const u16*)d_in[6],
                                         (const u16*)d_in[7], (const u16*)d_in[8], ws);
    k_gemm12<1><<<256, 512, G12_LDS, stream>>>(ws);
    k_gemm12<2><<<256, 512, G12_LDS, stream>>>(ws);
    k_gemm3<<<256, 512, G3_LDS, stream>>>(ws);
    k_gemm4<<<256, 256, G4_LDS, stream>>>(ws, (u16*)d_out);
}
```

```cpp
#include <hip/hip_runtime.h>
#include <hip/hip_bf16.h>
#include <stdint.h>

#define DEV __device__ __forceinline__
#define LAS __attribute__((address_space(3)))
#define GAS __attribute__((address_space(1)))
typedef unsigned int u32;
typedef unsigned short u16;
typedef unsigned long long u64;
typedef float f32x2 __attribute__((ext_vector_type(2)));
typedef float f32x4 __attribute__((ext_vector_type(4)));
typedef float f32x16 __attribute__((ext_vector_type(16)));
typedef u32 u32x2 __attribute__((ext_vector_type(2)));
typedef u32 u32x4 __attribute__((ext_vector_type(4)));
typedef short s16x4 __attribute__((ext_vector_type(4)));
typedef __bf16 bf16x2 __attribute__((ext_vector_type(2)));
typedef __bf16 bf16x8 __attribute__((ext_vector_type(8)));
typedef _Float16 f16x2 __attribute__((ext_vector_type(2)));
typedef _Float16 f16x8 __attribute__((ext_vector_type(8)));

#define TS(i)

constexpr int L = 2048, D = 512, NS = 128, F = 1792;

constexpr u32 WS_CTL = 0;
constexpr u32 WS_F1 = 1024;
constexpr u32 WS_GR = 16384;
constexpr u32 WS_F2 = 32768;
constexpr u32 WS_F3 = 65536;
constexpr u32 WS_F4 = 98304;
constexpr u32 WS_F5 = 131072;
constexpr u32 WS_WT3 = 163840;
constexpr u32 WS_WT4 = WS_WT3 + 512 * 1792 * 2;
constexpr u32 WS_HC = WS_WT4 + 512 * 1792 * 2;
constexpr u32 WS_HG = WS_HC + 2048 * 512 * 2;
constexpr u32 WS_X1 = WS_HG + 2048 * 512 * 2;
constexpr u32 WS_G = WS_X1 + 2048 * 512 * 2;
constexpr u32 WS_END = WS_G + 2048 * 1792 * 2;

__device__ const u32x4 g_zero16[4] = {};

constexpr int c_round_bf16(int Lg) {
    if (Lg < 256) return Lg;
    int e = 0;
    while ((Lg >> (e + 1)) != 0) ++e;
    int sh = e - 7, q = Lg >> sh, rem = Lg & ((1 << sh) - 1), half = 1 << (sh - 1);
    if (rem > half || (rem == half && (q & 1))) ++q;
    return q << sh;
}
constexpr int c_ci(int Lr) {
    if (Lr < 256) return Lr;
    int e = 0;
    while ((Lr >> (e + 1)) != 0) ++e;
    return 128 * (e - 6) + ((Lr >> (e - 7)) - 128);
}
constexpr int KC_ZERO = 644, KC_DUMMY = 648, KC_LEN = 656;
struct Tabs {
    u16 ci[3 * 64 * 4];
    u16 ex[256 * 12];
    constexpr Tabs() : ci(), ex() {
        for (int mt = 0; mt < 3; ++mt)
            for (int l = 0; l < 64; ++l)
                for (int e = 0; e < 4; ++e) {
                    int row = mt * 16 + (l >> 4) * 4 + e, j = l & 15, lrow = 0;
                    if (row < 16) lrow = (row & 7) + 128 * (row >> 3);
                    else if (row < 24) lrow = 256 + 2 * (row & 3) + 128 * ((row >> 2) & 1);
                    else if (row < 32) lrow = 512 + 4 * (row & 1) + 128 * ((row >> 1) & 3);
                    else if (row < 40) lrow = 1024 + 128 * (row & 7);
                    else lrow = 2048;
                    bool valid = row < 40 || (row == 40 && j == 0);
                    ci[(mt * 64 + l) * 4 + e] = (u16)(2 * (valid ? c_ci(lrow + 8 * j) : KC_DUMMY));
                }
        for (int c = 0; c < 256; ++c)
            for (int i = 0; i < 12; ++i) {
                int lag = 2047 - (8 * c + i);
                ex[c * 12 + i] = (u16)(2 * (lag < 0 ? KC_ZERO : c_ci(c_round_bf16(lag))));
            }
    }
};
__device__ const Tabs g_tabs = Tabs();

DEV float bf_lo(u32 v) { return __uint_as_float(v << 16); }
DEV float bf_hi(u32 v) { return __uint_as_float(v & 0xffff0000u); }
DEV float bf1(u16 v) { return __uint_as_float(((u32)v) << 16); }
DEV u32 pk_bf16(float a, float b) {
    f32x2 f = {a, b};
    bf16x2 r = __builtin_convertvector(f, bf16x2);
    return __builtin_bit_cast(u32, r);
}
DEV u32 pk_bf16v(f32x2 f) {
    bf16x2 r = __builtin_convertvector(f, bf16x2);
    return __builtin_bit_cast(u32, r);
}
DEV f32x2 unpk(u32 v) { return f32x2{bf_lo(v), bf_hi(v)}; }
DEV float rbf(float a) { return bf_lo(pk_bf16(a, 0.f)); }
DEV u32 pk_f16(float a, float b) {
    f32x2 f = {a, b};
    f16x2 r = __builtin_convertvector(f, f16x2);
    return __builtin_bit_cast(u32, r);
}
DEV float dot2bf(u32 a, u32 b, float c) {
    return __builtin_amdgcn_fdot2_f32_bf16(__builtin_bit_cast(bf16x2, a), __builtin_bit_cast(bf16x2, b), c, false);
}
DEV f32x16 mfma32(u32x4 a, u32x4 b, f32x16 c) {
    return __builtin_amdgcn_mfma_f32_32x32x16_bf16(__builtin_bit_cast(bf16x8, a), __builtin_bit_cast(bf16x8, b), c, 0, 0, 0);
}
DEV f32x4 mfma16h(u32x4 a, u32x4 b, f32x4 c) {
    return __builtin_amdgcn_mfma_f32_16x16x32_f16(__builtin_bit_cast(f16x8, a), __builtin_bit_cast(f16x8, b), c, 0, 0, 0);
}
DEV f32x2 gelu2(f32x2 x) {
    const float c1 = -2.0f * 0.7978845608028654f * 1.4426950408889634f;
    const float c3 = c1 * 0.044715f;
    f32x2 t = x * (x * x * c3 + c1);
    f32x2 e = {__builtin_amdgcn_exp2f(t.x), __builtin_amdgcn_exp2f(t.y)};
    e = e + 1.0f;
    f32x2 r = {__builtin_amdgcn_rcpf(e.x), __builtin_amdgcn_rcpf(e.y)};
    return x * r;
}
template <int CTRL, int RM>
DEV float dpp_f(float v) {
    return __int_as_float(__builtin_amdgcn_update_dpp(0, __float_as_int(v), CTRL, RM, 0xf, true));
}
DEV float wave_sum(float v) {
    v += dpp_f<0x111, 0xf>(v); v += dpp_f<0x112, 0xf>(v); v += dpp_f<0x114, 0xf>(v); v += dpp_f<0x118, 0xf>(v);
    v += dpp_f<0x142, 0xa>(v); v += dpp_f<0x143, 0xc>(v);
    return __int_as_float(__builtin_amdgcn_readlane(__float_as_int(v), 63));
}
DEV float wave_max(float v) {
    v = fmaxf(v, dpp_f<0x111, 0xf>(v)); v = fmaxf(v, dpp_f<0x112, 0xf>(v)); v = fmaxf(v, dpp_f<0x114, 0xf>(v));
    v = fmaxf(v, dpp_f<0x118, 0xf>(v)); v = fmaxf(v, dpp_f<0x142, 0xa>(v)); v = fmaxf(v, dpp_f<0x143, 0xc>(v));
    return __int_as_float(__builtin_amdgcn_readlane(__float_as_int(v), 63));
}
DEV float add_xor32(float v) {
    u32 u = __float_as_uint(v);
    auto sw = __builtin_amdgcn_permlane32_swap(u, u, false, false);
    u32 a = sw[0], b = sw[1];
    return __uint_as_float(a) + __uint_as_float(b);
}
#define WAIT_V(n) asm volatile("s_waitcnt vmcnt(" #n ")" ::: "memory")
#define FENCE() asm volatile("" ::: "memory")
#define LBAR() do { asm volatile("s_waitcnt lgkmcnt(0)" ::: "memory"); __builtin_amdgcn_s_barrier(); asm volatile("" ::: "memory"); } while (0)
#define PIN4(a) asm volatile("" : "+v"(a))

DEV void ln_consts(float mean, float var, float& m_bf, float& sd_bf) {
    m_bf = rbf(mean);
    float v = rbf(var);
    v = rbf(v + 1e-5f);
    sd_bf = rbf(__builtin_amdgcn_sqrtf(v));
}
DEV void dma_ws(u32x4 srd, u32 voff, u32 soff, u32 ldst) {
    asm volatile("s_mov_b32 m0, %0\n\ts_nop 0\n\tbuffer_load_dwordx4 %1, %2, %3 offen sc1 lds"
                 :: "s"(ldst), "v"(voff), "s"(srd), "s"(soff) : "memory");
}
DEV void dma_ws2(u32x4 srd, u32 voff, u32 soff, u32 ldst) {
    asm volatile("s_mov_b32 m0, %0\n\ts_nop 0\n\tbuffer_load_dwordx4 %1, %2, %3 offen sc1 lds\n\t"
                 "buffer_load_dwordx4 %1, %2, %3 offen offset:1024 sc1 lds"
                 :: "s"(ldst), "v"(voff), "s"(srd), "s"(soff) : "memory");
}
DEV void dma_ws4(u32x4 srd, u32 voff, u32 soff, u32 ldst) {
    asm volatile("s_mov_b32 m0, %0\n\ts_nop 0\n\tbuffer_load_dwordx4 %1, %2, %3 offen sc1 lds\n\t"
                 "buffer_load_dwordx4 %1, %2, %3 offen offset:1024 sc1 lds\n\t"
                 "buffer_load_dwordx4 %1, %2, %3 offen offset:2048 sc1 lds\n\t"
                 "buffer_load_dwordx4 %1, %2, %3 offen offset:3072 sc1 lds"
                 :: "s"(ldst), "v"(voff), "s"(srd), "s"(soff) : "memory");
}
DEV void dma_in(u32x4 srd, u32 voff, u32 soff, u32 ldst) {
    asm volatile("s_mov_b32 m0, %0\n\ts_nop 0\n\tbuffer_load_dwordx4 %1, %2, %3 offen lds"
                 :: "s"(ldst), "v"(voff), "s"(srd), "s"(soff) : "memory");
}
DEV void dma_g(const void* g, u32 ldst) {
    asm volatile("s_mov_b32 m0, %0\n\ts_nop 0\n\tglobal_load_lds_dwordx4 %1, off sc1"
                 :: "s"(ldst), "v"(g) : "memory");
}
DEV u32x4 make_srd(const void* p, u32 bytes) {
    u64 a = (u64)p;
    return u32x4{(u32)a, (u32)(a >> 32) & 0xffffu, bytes, 0x00020000u};
}
#define SCHED0() __builtin_amdgcn_sched_barrier(0)
DEV u32x4 tr_frag(const LAS char* p) {
    s16x4 a = __builtin_amdgcn_ds_read_tr16_b64_v4i16((LAS s16x4*)p);
    s16x4 b = __builtin_amdgcn_ds_read_tr16_b64_v4i16((LAS s16x4*)(p + 1024));
    u32x2 x = __builtin_bit_cast(u32x2, a), y = __builtin_bit_cast(u32x2, b);
    return u32x4{x[0], x[1], y[0], y[1]};
}

struct cplx { float x, y; };
DEV cplx cmul(cplx a, cplx b) {
    cplx r;
    asm("v_mul_f32 %0, %3, %5\n\tv_mul_f32 %1, %3, %4\n\tv_fma_f32 %0, %2, %4, -%0\n\tv_fma_f32 %1, %2, %5, %1"
        : "=&v"(r.x), "=&v"(r.y) : "v"(a.x), "v"(a.y), "v"(b.x), "v"(b.y));
    return r;
}
DEV cplx csq(cplx a) {
    cplx r;
    asm("v_mul_f32 %0, %3, %3\n\tv_mul_f32 %1, %2, %3\n\tv_fma_f32 %0, %2, %2, -%0\n\tv_add_f32 %1, %1, %1"
        : "=&v"(r.x), "=&v"(r.y) : "v"(a.x), "v"(a.y));
    return r;
}
DEV cplx cexp_pow2(float tr, float wh, float wl, float p2) {
    float mag = __builtin_amdgcn_exp2f(tr * p2);
    float ph = __builtin_amdgcn_fractf(wh * p2) + wl * p2;
    return cplx{mag * __builtin_amdgcn_cosf(ph), mag * __builtin_amdgcn_sinf(ph)};
}
template <int CTRL, int RM>
DEV u32 dpp_u(u32 v) { return (u32)__builtin_amdgcn_update_dpp(0, (int)v, CTRL, RM, 0xf, true); }
DEV u32 wave_max_u(u32 v) {
    v = max(v, dpp_u<0x111, 0xf>(v)); v = max(v, dpp_u<0x112, 0xf>(v)); v = max(v, dpp_u<0x114, 0xf>(v));
    v = max(v, dpp_u<0x118, 0xf>(v)); v = max(v, dpp_u<0x142, 0xa>(v)); v = max(v, dpp_u<0x143, 0xc>(v));
    return (u32)__builtin_amdgcn_readlane((int)v, 63);
}
DEV float gelu_s(float d, float r, float ga, float gb) {
    float t = d * __builtin_fmaf(d * d, gb, ga);
    float e = __builtin_amdgcn_exp2f(t);
    return (d * r) * __builtin_amdgcn_rcpf(1.0f + e);
}
constexpr float GC1 = -2.0f * 0.7978845608028654f * 1.4426950408889634f, GC3 = GC1 * 0.044715f;

constexpr int TRS = 528;
constexpr int RCOPY = 4160;
constexpr int A_R = 0;
constexpr int A_U = 33280, UPAD = 240, ULEN = 2304;
constexpr int A_KC = A_U + 2 * ULEN * 2;
constexpr int A_X = 45312, A_XCH = 41 * TRS;
constexpr int A_Y = A_X + 2 * A_XCH, A_YCH = 16 * TRS;
constexpr int A_SCR = 107520;
constexpr int A_YP = 45312;
constexpr int A_W = 98304;
constexpr int LDS_BYTES = 163840;
constexpr int G_ACT = 0, G_RED = 65536, G_PS = 81920, G_CS = 86016, G_SPARE = 114688;
constexpr int SPIN_MAX = 1 << 18;

__global__ __launch_bounds__(512, 2) void k_mega(const u16* __restrict__ x, const u16* __restrict__ A_re,
                                                 const u16* __restrict__ A_im, const u16* __restrict__ B_re,
                                                 const u16* __restrict__ B_im, const u16* __restrict__ C_re,
                                                 const u16* __restrict__ C_im, const u16* __restrict__ log_dt,
                                                 const u16* __restrict__ Dskip, const u16* __restrict__ w1,
                                                 const u16* __restrict__ w2, const u16* __restrict__ w3,
                                                 const u16* __restrict__ w4, u16* __restrict__ out, char* ws) {
    extern __shared__ __attribute__((aligned(16))) char lds_raw[];
    LAS char* lds = (LAS char*)lds_raw;
    const int tid = threadIdx.x, lane = tid & 63, wave = __builtin_amdgcn_readfirstlane(tid >> 6);
    const int b = blockIdx.x, bq = b >> 3;
    const int cb = (b & 7) * 32 + bq, d0 = cb * 2;
    const int tt = (b & 7) * 4 + (bq >> 3), ct = bq & 7;
    const auto rs = __builtin_amdgcn_make_buffer_rsrc(ws, 0, WS_END, 0x00020000);
    const u32x4 srd_ws = make_srd(ws, WS_END), srd_w1 = make_srd(w1, D * D * 2), srd_w2 = make_srd(w2, D * D * 2);
    const u32 lbase = (u32)(size_t)lds;
    asm volatile("" :: "s"(x), "s"(A_re), "s"(A_im), "s"(B_re), "s"(B_im), "s"(C_re), "s"(C_im), "s"(log_dt), "s"(Dskip),
                 "s"(w1), "s"(w2), "s"(w3), "s"(w4), "s"(out), "s"(ws));
    TS(0);

    const u32 ep = *(const GAS u32*)(ws + WS_CTL) + 1u;
    u32x4 xr = *(const u32x4*)(x + (size_t)(b * 8 + wave) * D + lane * 8);
    const int kch = wave >> 2, knh = wave & 1, kpart = (wave >> 1) & 1;
    const int pidx = (d0 + kch) * NS + knh * 64 + lane;
    u16 p_are = A_re[pidx], p_aim = A_im[pidx], p_bre = B_re[pidx], p_bim = B_im[pidx], p_cre = C_re[pidx], p_cim = C_im[pidx];
    u16 o_bre = B_re[pidx ^ 64], o_bim = B_im[pidx ^ 64], o_cre = C_re[pidx ^ 64], o_cim = C_im[pidx ^ 64];
    u32 p_ldt = *(const u32*)(log_dt + d0);
    u32 p_dsk = *(const u32*)(Dskip + d0);
    u32x4 traw[2];
    u32 tdst = 0;
    {
        int pr = b * 7 + (wave < 7 ? wave : 0);
        const u16* src; int N;
        if (pr < 896) { int ntp = pr >> 5, ks = pr & 31; src = w3 + (size_t)(16 * ks) * F + ntp * 64; N = F;
                        tdst = WS_WT3 + ((ntp * 2) * 32 + ks) * 1024; }
        else { int p2 = pr - 896; int ntp = p2 / 112, ks = p2 - ntp * 112; src = w4 + (size_t)(16 * ks) * D + ntp * 64; N = D;
               tdst = WS_WT4 + ((ntp * 2) * 112 + ks) * 1024; }
#pragma unroll
        for (int i = 0; i < 2; ++i) traw[i] = *(const u32x4*)(src + (size_t)(8 * i + (lane >> 3)) * N + (lane & 7) * 8);
    }
    u32x2 tci = *(const u32x2*)(g_tabs.ci + ((wave & 3) < 3 ? (wave & 3) : 0) * 256 + lane * 4);
    u32x4 tex0 = *(const u32x4*)(g_tabs.ex + (tid & 255) * 12);
    u32x2 tex1 = *(const u32x2*)(g_tabs.ex + (tid & 255) * 12 + 8);

    FENCE();
    __builtin_amdgcn_s_barrier();
    FENCE();
    u32 xv[4];
    const u16* xcol = x + (size_t)(tid * 4) * D + d0;
#define GATHER(k) do { FENCE(); xv[k] = *(const u32*)(xcol + (k) * D); FENCE(); } while (0)
    GATHER(0);
    {
        f32x2 f[4];
#pragma unroll
        for (int i = 0; i < 4; ++i) f[i] = unpk(xr[i]);
        f32x2 s2 = (f[0] + f[1]) + (f[2] + f[3]);
        float mean = wave_sum(s2.x + s2.y) * (1.0f / D);
        f32x2 q2 = {0.f, 0.f};
#pragma unroll
        for (int i = 0; i < 4; ++i) { f32x2 dd = f[i] - mean; q2 += dd * dd; }
        float var = wave_sum(q2.x + q2.y) * (1.0f / D);
        float m, sd;
        ln_consts(mean, var, m, sd);
        u32 val = (__float_as_uint(m) >> 16) | (__float_as_uint(sd) & 0xffff0000u);
        if (lane == 0)
            __hip_atomic_store((GAS u64*)(ws + WS_GR) + (b * 8 + wave), ((u64)ep << 32) | val, __ATOMIC_RELAXED,
                               __HIP_MEMORY_SCOPE_AGENT);
    }
    TS(1);
    float inv_scale[2];
    {
        float dts[2] = {rbf(__expf(bf_lo(p_ldt))), rbf(__expf(bf_hi(p_ldt)))};
        float dt = kch ? dts[1] : dts[0];
        float zr = dt * bf1(p_are), zi = dt * bf1(p_aim);
        float tr = zr * 1.4426950408889634f;
        const float inv2pi_h = 0.15915494309189535f;
        const float inv2pi_l = (float)(0.15915494309189533576888 - (double)inv2pi_h);
        float wh = zi * inv2pi_h;
        float wl = __builtin_fmaf(zi, inv2pi_h, -wh) + zi * inv2pi_l;
        cplx CB = cmul(cplx{bf1(p_cre), bf1(p_cim)}, cplx{dt * bf1(p_bre), dt * bf1(p_bim)});
        cplx CO = cmul(cplx{bf1(o_cre), bf1(o_cim)}, cplx{dt * bf1(o_bre), dt * bf1(o_bim)});
        u32 mx = wave_max_u(max(max(__float_as_uint(CB.x) & 0x7fffffffu, __float_as_uint(CB.y) & 0x7fffffffu),
                                max(__float_as_uint(CO.x) & 0x7fffffffu, __float_as_uint(CO.y) & 0x7fffffffu)));
        int e = (int)(mx >> 23);
        e = e < 8 ? 8 : (e > 246 ? 246 : e);
        float sc = __uint_as_float((u32)(254 + 6 - e) << 23);
        CB.x *= sc; CB.y *= sc;
        if (lane == 0 && knh == 0 && kpart == 0) {
            *(LAS float*)(lds + A_KC + (kch * KC_LEN + 652) * 2) = __uint_as_float((u32)(e - 6) << 23);
            *(LAS u32*)(lds + A_KC + (kch * KC_LEN + KC_ZERO) * 2) = 0u;
        }
        GATHER(1);
        LAS char* Xb = lds + A_X + kch * A_XCH + (knh * 64 + lane) * 4;
        LAS char* Yb = lds + A_Y + kch * A_YCH + (knh * 64 + lane) * 4;
#define PUTX(row, v) *(LAS u32*)(Xb + (row) * TRS) = pk_f16((v).x, (v).y)
#define PUTY(row, v) *(LAS u32*)(Yb + (row) * TRS) = pk_f16((v).x, (v).y)
        if (kpart == 0) {
            cplx P1 = cexp_pow2(tr, wh, wl, 1.f), Q8 = cexp_pow2(tr, -wh, -wl, 8.f), P128 = cexp_pow2(tr, wh, wl, 128.f);
            cplx P2 = csq(P1), Q16 = csq(Q8), P4 = csq(P2), Q32 = csq(Q16), Q64 = csq(Q32);
            cplx Y[8];
            Y[0] = cplx{1.f, 0.f}; Y[1] = Q8; Y[2] = Q16; Y[3] = cmul(Q8, Q16); Y[4] = Q32;
            Y[5] = cmul(Q32, Q8); Y[6] = cmul(Q32, Q16); Y[7] = cmul(Q32, Y[3]);
#pragma unroll
            for (int j = 0; j < 8; ++j) PUTY(j, Y[j]);
            PUTY(8, Q64);
#pragma unroll
            for (int j = 1; j < 8; ++j) { cplx v = cmul(Q64, Y[j]); PUTY(8 + j, v); }
            cplx Xa[8];
            Xa[0] = CB; Xa[1] = cmul(CB, P1); Xa[2] = cmul(CB, P2); Xa[3] = cmul(Xa[2], P1);
#pragma unroll
            for (int j = 0; j < 4; ++j) Xa[4 + j] = cmul(Xa[j], P4);
#pragma unroll
            for (int j = 0; j < 8; ++j) { PUTX(j, Xa[j]); cplx v = cmul(Xa[j], P128); PUTX(8 + j, v); }
        } else {
            cplx P2 = cexp_pow2(tr, wh, wl, 2.f), P128 = cexp_pow2(tr, wh, wl, 128.f), P1024 = cexp_pow2(tr, wh, wl, 1024.f);
            cplx P4 = csq(P2), P256 = csq(P128), P2048 = csq(P1024), P512 = csq(P256);
            cplx X0 = CB, X2 = cmul(CB, P2), X4 = cmul(CB, P4), X6 = cmul(X2, P4);
            cplx X128 = cmul(CB, P128), X132 = cmul(X4, P128);
            cplx c0[4] = {cmul(X0, P256), cmul(X2, P256), cmul(X4, P256), cmul(X6, P256)};
            cplx c1[4];
#pragma unroll
            for (int j = 0; j < 4; ++j) c1[j] = cmul(c0[j], P128);
#pragma unroll
            for (int j = 0; j < 4; ++j) { PUTX(16 + j, c0[j]); PUTX(20 + j, c1[j]); }
            cplx dd[8] = {cmul(X0, P512), cmul(X4, P512), cmul(X128, P512), cmul(X132, P512),
                          cmul(c0[0], P512), cmul(c0[2], P512), cmul(c1[0], P512), cmul(c1[2], P512)};
#pragma unroll
            for (int j = 0; j < 8; ++j) PUTX(24 + j, dd[j]);
            cplx ee[8] = {X0, X128, c0[0], c1[0], dd[0], dd[2], dd[4], dd[6]};
#pragma unroll
            for (int j = 0; j < 8; ++j) { cplx v = cmul(ee[j], P1024); PUTX(32 + j, v); }
            cplx v = cmul(CB, P2048);
            PUTX(40, v);
        }
    }

    GATHER(2);
    const int trq = (lane & 15) >> 2, trp = lane & 3, trh = lane >> 5, trg = (lane >> 4) & 1;
    const int tr_off = (4 * trh + trq) * 128 + (((2 * trg + (trp >> 1)) ^ ((trq >> 1) << 2)) << 4) + 8 * (trp & 1);
    const int dma_sw = ((lane & 7) ^ (((lane >> 4) & 1) << 2)) * 8;
    {
        LAS char* scr = lds + A_SCR + wave * 2048;
#pragma unroll
        for (int i = 0; i < 2; ++i)
            *(LAS u32x4*)(scr + i * 1024 + (lane >> 3) * 128 + (((lane & 7) ^ (((lane >> 4) & 1) << 2)) << 4)) = traw[i];
        u32x4 f0 = tr_frag(scr + tr_off), f1 = tr_frag(scr + (tr_off ^ 64));
        if (wave < 7) {
            const u32 nstride = tdst < WS_WT4 ? 32 * 1024 : 112 * 1024;
            __builtin_amdgcn_raw_buffer_store_b128(f0, rs, tdst + lane * 16, 0, 16);
            __builtin_amdgcn_raw_buffer_store_b128(f1, rs, tdst + nstride + lane * 16, 0, 16);
        }
    }
    LBAR();
    TS(2);
    GATHER(3);
    if ((wave & 3) < 3) {
        const int mt = wave & 3, r = lane & 15, g = lane >> 4;
        const LAS char* Xb = lds + A_X + kch * A_XCH + (mt * 16 + r) * TRS + g * 16;
        const LAS char* Yb = lds + A_Y + kch * A_YCH + r * TRS + g * 16;
        f32x4 acc0 = {0, 0, 0, 0}, acc1 = {0, 0, 0, 0};
#pragma unroll
        for (int ks = 0; ks < 8; ks += 2) {
            acc0 = mfma16h(*(const LAS u32x4*)(Xb + ks * 64), *(const LAS u32x4*)(Yb + ks * 64), acc0);
            acc1 = mfma16h(*(const LAS u32x4*)(Xb + ks * 64 + 64), *(const LAS u32x4*)(Yb + ks * 64 + 64), acc1);
        }
        acc0 += acc1;
        u32 k01 = pk_f16(acc0[0], acc0[1]), k23 = pk_f16(acc0[2], acc0[3]);
        LAS char* Kc = lds + A_KC + kch * KC_LEN * 2;
        *(LAS u16*)(Kc + (tci[0] & 0xffff)) = (u16)k01;
        *(LAS u16*)(Kc + (tci[0] >> 16)) = (u16)(k01 >> 16);
        *(LAS u16*)(Kc + (tci[1] & 0xffff)) = (u16)k23;
        *(LAS u16*)(Kc + (tci[1] >> 16)) = (u16)(k23 >> 16);
    }
    LBAR();
    TS(3);
    u32x4 g0 = __builtin_amdgcn_raw_buffer_load_b128(rs, WS_GR + tid * 32, 0, 16);
    u32x4 g1 = __builtin_amdgcn_raw_buffer_load_b128(rs, WS_GR + tid * 32 + 16, 0, 16);
    FENCE();
    {
        const int ch = tid >> 8, c = tid & 255;
        const LAS char* Kc = lds + A_KC + ch * KC_LEN * 2;
        u32 pe[6];
        u32 tw[6] = {tex0[0], tex0[1], tex0[2], tex0[3], tex1[0], tex1[1]};
#pragma unroll
        for (int i = 0; i < 6; ++i) {
            u32 lo = *(const LAS u16*)(Kc + (tw[i] & 0xffff));
            u32 hi = *(const LAS u16*)(Kc + (tw[i] >> 16));
            pe[i] = lo | (hi << 16);
        }
        u32 po[5];
#pragma unroll
        for (int i = 0; i < 5; ++i) po[i] = __builtin_amdgcn_alignbit(pe[i + 1], pe[i], 16);
        inv_scale[0] = *(const LAS float*)(lds + A_KC + 652 * 2);
        inv_scale[1] = *(const LAS float*)(lds + A_KC + (KC_LEN + 652) * 2);
        LAS char* Rb = lds + A_R + ch * 4 * RCOPY + c * 16;
        *(LAS u32x4*)(Rb + 0 * RCOPY) = u32x4{pe[0], pe[1], pe[2], pe[3]};
        *(LAS u32x4*)(Rb + 1 * RCOPY) = u32x4{po[0], po[1], po[2], po[3]};
        *(LAS u32x4*)(Rb + 2 * RCOPY) = u32x4{pe[1], pe[2], pe[3], pe[4]};
        *(LAS u32x4*)(Rb + 3 * RCOPY) = u32x4{po[1], po[2], po[3], po[4]};
        if (tid < 16) *(LAS u32x4*)(lds + A_R + (tid >> 1) * RCOPY + 4096 + (tid & 1) * 16) = u32x4{0, 0, 0, 0};
        if (tid >= 64 && tid < 124) {
            *(LAS u32x2*)(lds + A_U + (tid - 64) * 8) = u32x2{0, 0};
            *(LAS u32x2*)(lds + A_U + ULEN * 2 + (tid - 64) * 8) = u32x2{0, 0};
        }
    }

    f32x2 ud[4];
    {
        for (int spin = 0; spin < SPIN_MAX; ++spin) {
            bool ok = g0[1] == ep && g0[3] == ep && g1[1] == ep && g1[3] == ep;
            if (__all(ok)) break;
            __builtin_amdgcn_s_sleep(1);
            FENCE();
            g0 = __builtin_amdgcn_raw_buffer_load_b128(rs, WS_GR + tid * 32, 0, 16);
            g1 = __builtin_amdgcn_raw_buffer_load_b128(rs, WS_GR + tid * 32 + 16, 0, 16);
        }

        u32 st[4] = {g0[0], g0[2], g1[0], g1[2]};
        f32x2 dsk = unpk(p_dsk);
        f32x2 u[4];
#pragma unroll
        for (int k = 0; k < 4; ++k) {
            float m = bf_lo(st[k]), rsd = __builtin_amdgcn_rcpf(bf_hi(st[k]));
            f32x2 dlt = unpk(pk_bf16v(unpk(xv[k]) - m));
            u[k] = unpk(pk_bf16v(dlt * rsd));
            ud[k] = unpk(pk_bf16v(u[k] * dsk));
        }
        *(LAS u32x2*)(lds + A_U + (UPAD + tid * 4) * 2) = u32x2{pk_f16(u[0].x, u[1].x), pk_f16(u[2].x, u[3].x)};
        *(LAS u32x2*)(lds + A_U + ULEN * 2 + (UPAD + tid * 4) * 2) = u32x2{pk_f16(u[0].y, u[1].y), pk_f16(u[2].y, u[3].y)};
    }
    const u32 img_voff = (lane >> 3) * (D * 2) + ct * 128 + dma_sw * 2;
    LBAR();
    TS(4);
    {
        const int ch = wave >> 2, rq = wave & 3;
        const int r = lane & 15, g = lane >> 4;
        const int o0 = 15 - r + 8 * g;
        const LAS char* Ab = lds + A_R + ch * 4 * RCOPY + (o0 & 3) * RCOPY + (o0 & ~3) * 2 + (6 - rq * 2) * 64;
        const LAS char* Bb = lds + A_U + ch * ULEN * 2 + (16 * r + 8 * g) * 2 + (6 - rq * 2) * 64;
        f32x4 acc[8];
#pragma unroll
        for (int i = 0; i < 8; ++i) acc[i] = f32x4{0, 0, 0, 0};
        u32x4 A0[8], B0[8], A1[8], B1[8];
#define LD1(AF, BF, a, rr) do { \
            u32x2 lo_ = *(const volatile LAS u32x2*)(Ab + (1 - (rr)) * 64 + (7 - (a)) * 512); \
            u32x2 hi_ = *(const volatile LAS u32x2*)(Ab + (1 - (rr)) * 64 + (7 - (a)) * 512 + 8); \
            AF[a] = u32x4{lo_[0], lo_[1], hi_[0], hi_[1]}; \
            BF[a] = *(const LAS u32x4*)(Bb + (1 - (rr)) * 64 + (a) * 512); } while (0)
#define GRP(AF, BF, m) do { _Pragma("unroll") for (int o_ = 0; o_ <= (m); ++o_) if (o_ + (m) <= 7) { \
            acc[o_ + (m)] = mfma16h(AF[m], BF[o_], acc[o_ + (m)]); \
            if (o_ != (m)) acc[o_ + (m)] = mfma16h(AF[o_], BF[m], acc[o_ + (m)]); } } while (0)
        LD1(A0, B0, 0, 0); LD1(A0, B0, 1, 0); LD1(A0, B0, 2, 0); LD1(A0, B0, 3, 0);
        SCHED0();
        GRP(A0, B0, 0); LD1(A0, B0, 4, 0); SCHED0();
        GRP(A0, B0, 1); LD1(A0, B0, 5, 0); SCHED0();
        GRP(A0, B0, 2); LD1(A0, B0, 6, 0); LD1(A0, B0, 7, 0); SCHED0();
        GRP(A0, B0, 3); LD1(A1, B1, 0, 1); LD1(A1, B1, 1, 1); SCHED0();
        GRP(A0, B0, 4); LD1(A1, B1, 2, 1); LD1(A1, B1, 3, 1); SCHED0();
        GRP(A0, B0, 5); LD1(A1, B1, 4, 1); LD1(A1, B1, 5, 1); SCHED0();
        GRP(A0, B0, 6); LD1(A1, B1, 6, 1); SCHED0();
        GRP(A0, B0, 7); LD1(A1, B1, 7, 1); SCHED0();

        GRP(A1, B1, 0); GRP(A1, B1, 1); GRP(A1, B1, 2); GRP(A1, B1, 3);
        GRP(A1, B1, 4); GRP(A1, B1, 5); GRP(A1, B1, 6); GRP(A1, B1, 7);
        asm volatile("" :: "v"(acc[7]), "v"(acc[0]));

        LAS char* Yp = lds + A_YP + (rq * 2 + ch) * 8192 + g * 256 + ((r ^ (4 * g)) << 4);
#pragma unroll
        for (int c = 0; c < 8; ++c) *(LAS f32x4*)(Yp + 1024 * c) = acc[c];
    }
    LBAR();
    TS(5);
    {
        const LAS char* Yp = lds + A_YP + wave * 1024 + (lane & 3) * 256 + (((lane >> 2) ^ (4 * (lane & 3))) << 4);
        f32x4 y0 = ((*(const LAS f32x4*)(Yp) + *(const LAS f32x4*)(Yp + 16384)) +
                    (*(const LAS f32x4*)(Yp + 32768) + *(const LAS f32x4*)(Yp + 49152))) * inv_scale[0];
        f32x4 y1 = ((*(const LAS f32x4*)(Yp + 8192) + *(const LAS f32x4*)(Yp + 24576)) +
                    (*(const LAS f32x4*)(Yp + 40960) + *(const LAS f32x4*)(Yp + 57344))) * inv_scale[1];
        u32 hp[4];
#pragma unroll
        for (int k = 0; k < 4; ++k) {
            f32x2 yy = unpk(pk_bf16(y0[k], y1[k]));
            f32x2 a = unpk(pk_bf16v(yy + ud[k]));
            hp[k] = pk_bf16v(unpk(xv[k]) + a);
        }
        u32 c00 = __builtin_amdgcn_perm(hp[1], hp[0], 0x05040100), c01 = __builtin_amdgcn_perm(hp[3], hp[2], 0x05040100);
        u32 c10 = __builtin_amdgcn_perm(hp[1], hp[0], 0x07060302), c11 = __builtin_amdgcn_perm(hp[3], hp[2], 0x07060302);
        const bool odd = lane & 1;
        u32 s0 = odd ? c00 : c10, s1 = odd ? c01 : c11;
        u32 r0 = __builtin_amdgcn_update_dpp(0, s0, 0xB1, 0xf, 0xf, true);
        u32 r1 = __builtin_amdgcn_update_dpp(0, s1, 0xB1, 0xf, 0xf, true);
        u32x4 o = odd ? u32x4{r0, r1, c10, c11} : u32x4{c00, c01, r0, r1};
        __builtin_amdgcn_raw_buffer_store_b128(o, rs, WS_HC + (tid >> 4) * 65536 + (d0 + (odd ? 1 : 0)) * 128 + ((tid >> 1) & 7) * 16, 0, 16);
    }
    LBAR();
#pragma unroll
    for (int i = 0; i < 8; ++i) dma_in(srd_w1, img_voff, (wave * 8 + i) * 8 * D * 2, lbase + G_ACT + (wave * 8 + i) * 1024);
    WAIT_V(8);
    LBAR();
    if (tid == 0) __hip_atomic_store((GAS u32*)(ws + WS_F1) + b, ep, __ATOMIC_RELAXED, __HIP_MEMORY_SCOPE_AGENT);
    TS(6);
#pragma unroll
    for (int i = 0; i < 8; ++i) dma_in(srd_w2, img_voff, (wave * 8 + i) * 8 * D * 2, lbase + A_W + (wave * 8 + i) * 1024);
    WAIT_V(8);
    LBAR();

    const int tq = wave & 1, nq = (wave >> 1) & 1, kh = wave >> 2;
    const int r32 = lane & 31, h32 = lane >> 5;
    const u32 ones = 0x3f803f80u;
    const u32x4 ones4 = {ones, ones, ones, ones};
    u32x4 wf[32];
    f32x16 cs = {};
    {
        const LAS char* WbE = lds + G_ACT + kh * 8192 + (tr_off ^ (nq << 6)) + nq * 2048;
        const LAS char* WbO = WbE - nq * 4096;
#pragma unroll
        for (int i = 0; i < 16; ++i) wf[i] = tr_frag(((i & 1) ? WbO : WbE) + ((i >> 2) * 8 + (i & 3)) * 2048);
#pragma unroll
        for (int i = 0; i < 16; ++i) cs = mfma32(wf[i], ones4, cs);
        if (r32 == 0 && tq == 0) {
#pragma unroll
            for (int i = 0; i < 4; ++i)
                *(LAS f32x4*)(lds + G_CS + ((nq * 2 + kh) * 2 + h32) * 64 + i * 16) = f32x4{cs[4 * i], cs[4 * i + 1], cs[4 * i + 2], cs[4 * i + 3]};
        }
    }
    if (wave == 7) {
        for (int spin = 0; spin < SPIN_MAX; ++spin) {
            FENCE();
            u32x4 f = __builtin_amdgcn_raw_buffer_load_b128(rs, WS_F1 + lane * 16, 0, 16);
            if (__all(f[0] == ep && f[1] == ep && f[2] == ep && f[3] == ep)) break;
            __builtin_amdgcn_s_sleep(1);
        }
        if (b == 0 && lane == 0) *(GAS u32*)(ws + WS_CTL) = ep;
    }
    LBAR();
    TS(7);
    {
        const u32 voff = (lane >> 3) * 128 + dma_sw * 2;
#pragma unroll
        for (int c = 0; c < 4; ++c)
            dma_ws2(srd_ws, voff, WS_HC + tt * 65536 + (8 * c + wave) * 2048, lbase + G_ACT + (8 * c + wave) * 2048);
    }
    float csv[8];
    {
        const LAS char* p = lds + G_CS + (nq * 4 + h32) * 64 + kh * 32;
        f32x4 a0 = *(const LAS f32x4*)(p), a1 = *(const LAS f32x4*)(p + 16);
        f32x4 b0 = *(const LAS f32x4*)(p + 128), b1 = *(const LAS f32x4*)(p + 144);
#pragma unroll
        for (int e = 0; e < 4; ++e) { csv[e] = a0[e] + b0[e]; csv[4 + e] = a1[e] + b1[e]; }
    }
    const u32 w3off = WS_WT3 + (ct * 7 + (wave < 7 ? wave : 0)) * 32768 + lane * 16;
    u32x4 w3f[32];
#define W3LOAD(q) do { FENCE(); if (wave < 7) { _Pragma("unroll") for (int i_ = 8 * (q); i_ < 8 * (q) + 8; ++i_) \
        w3f[i_] = __builtin_amdgcn_raw_buffer_load_b128(rs, w3off + i_ * 1024, 0, 16); } FENCE(); } while (0)
    u32x4 hres, x1res;
    {
        f32x16 acc = {};
        u32x4 own[2];
        float s1a = 0.f, s1b = 0.f, q1a = 0.f, q1b = 0.f;
        const LAS char* Ab = lds + G_ACT + (tr_off ^ (tq << 6));
        const LAS char* AbE = Ab + kh * 8192 + nq * 2048;
        const LAS char* AbO = AbE - nq * 4096;
#pragma unroll
        for (int c = 0; c < 4; ++c) {
            if (c == 0) WAIT_V(6); else if (c == 1) WAIT_V(4); else if (c == 2) WAIT_V(2); else WAIT_V(0);
            LBAR();
            if (c == 3) { W3LOAD(0); }
#pragma unroll
            for (int kk = 0; kk < 4; ++kk) {
                u32x4 bb = tr_frag(((kk & 1) ? AbO : AbE) + (8 * c + kk) * 2048);
                acc = mfma32(wf[4 * c + kk], bb, acc);
                if ((kk & 1) == 0) own[kk >> 1] = bb;
            }
#pragma unroll
            for (int i = 0; i < 2; ++i) {
                const u32x4 v = own[i];
                s1a = dot2bf(v[0], ones, s1a); s1b = dot2bf(v[1], ones, s1b);
                q1a = dot2bf(v[0], v[0], q1a); q1b = dot2bf(v[1], v[1], q1b);
                s1a = dot2bf(v[2], ones, s1a); s1b = dot2bf(v[3], ones, s1b);
                q1a = dot2bf(v[2], v[2], q1a); q1b = dot2bf(v[3], v[3], q1b);
            }
        }
        hres = tr_frag(Ab + (4 * ct + 2 * nq + kh) * 2048);
        TS(8);
        {
            LAS char* p = lds + G_RED + wave * 2048 + lane * 16;
            if (kh == 0) { *(LAS f32x4*)(p) = f32x4{acc[8], acc[9], acc[10], acc[11]}; *(LAS f32x4*)(p + 1024) = f32x4{acc[12], acc[13], acc[14], acc[15]}; }
            else { *(LAS f32x4*)(p) = f32x4{acc[0], acc[1], acc[2], acc[3]}; *(LAS f32x4*)(p + 1024) = f32x4{acc[4], acc[5], acc[6], acc[7]}; }
        }
        float s1 = add_xor32(s1a + s1b), q1 = add_xor32(q1a + q1b);
        LAS char* ps = lds + G_PS + (tq * 32 + r32) * 32;
        if (h32 == 0) *(LAS f32x2*)(ps + (nq * 2 + kh) * 8) = f32x2{s1, q1};
        LBAR();
        TS(9);
        float fin[8];
        {
            const LAS char* p = lds + G_RED + (wave ^ 4) * 2048 + lane * 16;
            f32x4 a0 = *(const LAS f32x4*)(p), a1 = *(const LAS f32x4*)(p + 1024);
#pragma unroll
            for (int e = 0; e < 4; ++e) {
                fin[e] = a0[e] + (kh ? acc[8 + e] : acc[e]);
                fin[4 + e] = a1[e] + (kh ? acc[12 + e] : acc[4 + e]);
            }
        }
        float m1, sd1, m2, sd2;
        f32x4 p0 = *(const LAS f32x4*)(ps), p1 = *(const LAS f32x4*)(ps + 16);
        const float mean = ((p0[0] + p0[2]) + (p1[0] + p1[2])) * (1.0f / 512);
        const float msq = ((p0[1] + p0[3]) + (p1[1] + p1[3])) * (1.0f / 512);
        const float var = fmaxf(msq - mean * mean, 0.f);
        ln_consts(mean, var, m1, sd1);
        const float r1 = __builtin_amdgcn_rcpf(sd1), nb1 = -m1 * r1;
        ln_consts((mean - m1) * r1, var * r1 * r1, m2, sd2);
        const float alpha = r1 * __builtin_amdgcn_rcpf(sd2);
        const float mu = m1 + m2 * sd1;
        const float ga = GC1 * alpha, gb = GC3 * alpha * alpha * alpha;
        u32x4 o;
#pragma unroll
        for (int e = 0; e < 4; ++e) {
            float d0_ = __builtin_fmaf(-mu, csv[2 * e], fin[2 * e]), d1_ = __builtin_fmaf(-mu, csv[2 * e + 1], fin[2 * e + 1]);
            o[e] = pk_bf16(gelu_s(d0_, alpha, ga, gb), gelu_s(d1_, alpha, ga, gb));
        }
        __builtin_amdgcn_raw_buffer_store_b128(o, rs, WS_HG + (((tt * 2 + tq) * 32 + 4 * ct + 2 * nq + kh) * 64 + lane) * 16, 0, 16);
#pragma unroll
        for (int e = 0; e < 4; ++e)
            x1res[e] = pk_bf16(__builtin_fmaf(bf_lo(hres[e]), r1, nb1), __builtin_fmaf(bf_hi(hres[e]), r1, nb1));
    }
    WAIT_V(0);
    LBAR();
    if (tid == 0) __hip_atomic_store((GAS u32*)(ws + WS_F2) + (tt * 8 + ct) * 32, ep, __ATOMIC_RELAXED, __HIP_MEMORY_SCOPE_AGENT);
    W3LOAD(1);
    TS(10);

    {
        const LAS char* Wb = lds + A_W + kh * 8192 + (tr_off ^ (nq << 6));
#pragma unroll
        for (int i = 0; i < 16; ++i) wf[i] = tr_frag(Wb + ((i >> 2) * 8 + (i & 3)) * 2048);
    }
    if (wave == 7) {
        const int tl = tt - 1 + (lane >> 3);
        const bool act = lane < 24 && tl >= 0 && tl < 32;
        const GAS u32* fp = (const GAS u32*)(ws + WS_F2) + ((act ? tl : tt) * 8 + (lane & 7)) * 32;
        for (int spin = 0; spin < SPIN_MAX; ++spin) {
            u32 f = __hip_atomic_load(fp, __ATOMIC_RELAXED, __HIP_MEMORY_SCOPE_AGENT);
            if (__all(f == ep)) break;
            __builtin_amdgcn_s_sleep(1);
        }
    }
    LBAR();
    TS(11);
#pragma unroll
    for (int c = 0; c < 4; ++c) {
        const int ks = 8 * c + wave;
        const int sh = c == 0 ? -1 : (c == 1 ? 1 : (c == 2 ? -2 : 2));
#pragma unroll
        for (int i = 0; i < 2; ++i) {
            const int t = tt * 64 + i * 32 + r32 + sh;
            const bool ok = (unsigned)t < (unsigned)L;
            const char* src = ok ? ws + WS_HG + ((((t >> 5) * 32 + ks) * 64) + h32 * 32 + (t & 31)) * 16 : (const char*)g_zero16;
            dma_g(src, lbase + G_ACT + (i * 32 + ks) * 1024);
        }
    }
    {
        f32x16 acc = {};
#pragma unroll
        for (int c = 0; c < 4; ++c) {
            if (c == 0) WAIT_V(6); else if (c == 1) WAIT_V(4); else if (c == 2) WAIT_V(2); else WAIT_V(0);
            LBAR();
            if (c == 3) { W3LOAD(2); }
#pragma unroll
            for (int kk = 0; kk < 4; ++kk) {
                const int ks = 8 * c + 4 * kh + kk;
                u32x4 bb = *(const LAS u32x4*)(lds + G_ACT + (tq * 32 + ks) * 1024 + lane * 16);
                acc = mfma32(wf[4 * c + kk], bb, acc);
            }
        }
        {
            LAS char* p = lds + G_RED + wave * 2048 + lane * 16;
            if (kh == 0) { *(LAS f32x4*)(p) = f32x4{acc[8], acc[9], acc[10], acc[11]}; *(LAS f32x4*)(p + 1024) = f32x4{acc[12], acc[13], acc[14], acc[15]}; }
            else { *(LAS f32x4*)(p) = f32x4{acc[0], acc[1], acc[2], acc[3]}; *(LAS f32x4*)(p + 1024) = f32x4{acc[4], acc[5], acc[6], acc[7]}; }
        }
        LBAR();
        TS(12);
        const LAS char* p = lds + G_RED + (wave ^ 4) * 2048 + lane * 16;
        f32x4 a0 = *(const LAS f32x4*)(p), a1 = *(const LAS f32x4*)(p + 1024);
        float fin[8];
#pragma unroll
        for (int e = 0; e < 4; ++e) {
            fin[e] = a0[e] + (kh ? acc[8 + e] : acc[e]);
            fin[4 + e] = a1[e] + (kh ? acc[12 + e] : acc[4 + e]);
        }
#pragma unroll
        for (int e = 0; e < 4; ++e)
            x1res[e] = pk_bf16v(unpk(x1res[e]) + unpk(pk_bf16(fin[2 * e], fin[2 * e + 1])));
        __builtin_amdgcn_raw_buffer_store_b128(x1res, rs, WS_X1 + (((tt * 2 + tq) * 32 + 4 * ct + 2 * nq + kh) * 64 + lane) * 16, 0, 16);
        *(LAS u32x4*)(lds + G_SPARE + wave * 2048 + lane * 16) = x1res;
        *(LAS u32x4*)(lds + G_SPARE + wave * 2048 + 1024 + lane * 16) = hres;
    }
    WAIT_V(0);
    LBAR();
    if (tid == 0) __hip_atomic_store((GAS u32*)(ws + WS_F3) + (tt * 8 + ct) * 32, ep, __ATOMIC_RELAXED, __HIP_MEMORY_SCOPE_AGENT);
    W3LOAD(3);
    TS(13);

    const int kq = 2 * tq + kh;
    const u32 w4off = WS_WT4 + ((ct * 2 + nq) * 112 + kq * 28) * 1024 + lane * 16;
    u32x4 w4f[28];
    const GAS u32* f4p = (const GAS u32*)(ws + WS_F4) + (tt * 8 + (lane & 7)) * 32;
#define ACT3_DMA() do { \
        dma_ws4(srd_ws, lane * 16, WS_X1 + tt * 65536 + wave * 4096, lbase + G_ACT + wave * 4096); \
        dma_ws4(srd_ws, lane * 16, WS_X1 + tt * 65536 + 32768 + wave * 4096, lbase + G_ACT + 32768 + wave * 4096); } while (0)
#define POLL(ptr) do { for (int spin_ = 0; spin_ < SPIN_MAX; ++spin_) { \
        u32 f_ = __hip_atomic_load((ptr), __ATOMIC_RELAXED, __HIP_MEMORY_SCOPE_AGENT); \
        if (__all(f_ == ep)) break; \
        __builtin_amdgcn_s_sleep(1); } } while (0)
    if (wave == 7) {
        POLL((const GAS u32*)(ws + WS_F3) + (tt * 8 + (lane & 7)) * 32);
        LBAR();
        ACT3_DMA();
#pragma unroll
        for (int i = 0; i < 2; ++i) {
            if (i == 0) WAIT_V(4); else WAIT_V(28);
            LBAR();
            if (i == 0) {
#pragma unroll
                for (int j = 0; j < 28; ++j) w4f[j] = __builtin_amdgcn_raw_buffer_load_b128(rs, w4off + j * 1024, 0, 16);
                FENCE();
            }
            float sa = 0.f, sb = 0.f, qa = 0.f, qb = 0.f;
            u32x4 vq[6];
#pragma unroll
            for (int j = 0; j < 6; ++j) vq[j] = *(const LAS u32x4*)(lds + G_ACT + (i * 32 + j) * 1024 + lane * 16);
#pragma unroll
            for (int ks = 0; ks < 32; ++ks) {
                const u32x4 v = vq[ks % 6];
                sa = dot2bf(v[0], ones, sa); sb = dot2bf(v[1], ones, sb);
                qa = dot2bf(v[0], v[0], qa); qb = dot2bf(v[1], v[1], qb);
                sa = dot2bf(v[2], ones, sa); sb = dot2bf(v[3], ones, sb);
                qa = dot2bf(v[2], v[2], qa); qb = dot2bf(v[3], v[3], qb);
                if (ks + 6 < 32) vq[ks % 6] = *(const LAS u32x4*)(lds + G_ACT + (i * 32 + ks + 6) * 1024 + lane * 16);
                SCHED0();
            }
            float mean = add_xor32(sa + sb) * (1.0f / 512), msq = add_xor32(qa + qb) * (1.0f / 512);
            float m, sd;
            ln_consts(mean, fmaxf(msq - mean * mean, 0.f), m, sd);
            if (h32 == 0) *(LAS f32x2*)(lds + G_PS + (i * 32 + r32) * 8) = f32x2{__builtin_amdgcn_rcpf(sd), m};
        }
        LBAR();
        POLL(f4p);
        LBAR();
    } else {
        cs = f32x16{};
#pragma unroll
        for (int i = 0; i < 32; ++i) cs = mfma32(w3f[i], ones4, cs);
        if (r32 == 0) {
#pragma unroll
            for (int i = 0; i < 4; ++i)
                *(LAS f32x4*)(lds + G_CS + (wave * 2 + h32) * 64 + i * 16) = f32x4{cs[4 * i], cs[4 * i + 1], cs[4 * i + 2], cs[4 * i + 3]};
        }
        LBAR();
        TS(14);
        ACT3_DMA();
        const LAS char* Ab = lds + G_ACT + lane * 16;
        const u32 gdst = WS_G + ((tt * 2 * 112 + (ct * 7 + wave) * 2) * 64 + lane) * 16;
        f32x16 acc0 = {}, acc1 = {};
        u32x4 bq[4];
        WAIT_V(4);
        LBAR();
        TS(30);
#pragma unroll
        for (int i = 0; i < 11; ++i) w4f[i] = __builtin_amdgcn_raw_buffer_load_b128(rs, w4off + i * 1024, 0, 16);
        FENCE();
#pragma unroll
        for (int i = 0; i < 4; ++i) bq[i] = *(const LAS u32x4*)(Ab + i * 1024);
#pragma unroll
        for (int ks = 0; ks < 32; ++ks) {
            acc0 = mfma32(w3f[ks], bq[ks & 3], acc0);
            if (ks + 4 < 32) bq[ks & 3] = *(const LAS u32x4*)(Ab + (ks + 4) * 1024);
            SCHED0();
        }
        TS(31);
        WAIT_V(11);
        LBAR();
        TS(15);
#pragma unroll
        for (int i = 0; i < 4; ++i) bq[i] = *(const LAS u32x4*)(Ab + (32 + i) * 1024);
#pragma unroll
        for (int i = 0; i < 4; ++i) {
            f32x4 c4 = *(const LAS f32x4*)(lds + G_CS + (wave * 2 + h32) * 64 + i * 16);
            cs[4 * i] = c4[0]; cs[4 * i + 1] = c4[1]; cs[4 * i + 2] = c4[2]; cs[4 * i + 3] = c4[3];
        }
        {
            const f32x2 st = *(const LAS f32x2*)(lds + G_PS + r32 * 8);
            const float ga = GC1 * st.x, gb = GC3 * st.x * st.x * st.x;
            float g[16];
            SCHED0();
#pragma unroll
            for (int k4 = 0; k4 < 32; k4 += 4) {
#pragma unroll
                for (int ks = k4; ks < k4 + 4; ++ks) {
                    acc1 = mfma32(w3f[ks], bq[ks & 3], acc1);
                    if (ks + 4 < 32) bq[ks & 3] = *(const LAS u32x4*)(Ab + (32 + ks + 4) * 1024);
                    if (ks < 16) g[ks] = gelu_s(__builtin_fmaf(-st.y, cs[ks], acc0[ks]), st.x, ga, gb);
                    if (ks >= 15) w4f[11 + ks - 15] = __builtin_amdgcn_raw_buffer_load_b128(rs, w4off + (11 + ks - 15) * 1024, 0, 16);
                }
                if (k4 == 8 || k4 == 16) {
                    const int o = k4 - 8;
                    u32x4 v = {pk_bf16(g[o], g[o + 1]), pk_bf16(g[o + 2], g[o + 3]), pk_bf16(g[o + 4], g[o + 5]), pk_bf16(g[o + 6], g[o + 7])};
                    __builtin_amdgcn_raw_buffer_store_b128(v, rs, gdst + (o >> 3) * 1024, 0, 16);
                }
#pragma unroll
                for (int q = 0; q < 4; ++q) {
                    __builtin_amdgcn_sched_group_barrier(0x008, 1, 0);
                    __builtin_amdgcn_sched_group_barrier(0x100, 1, 0);
                    __builtin_amdgcn_sched_group_barrier(0x002, 10, 0);
                    __builtin_amdgcn_sched_group_barrier(0x010, 1, 0);
                }
                SCHED0();
            }
        }
        WAIT_V(16);
        LBAR();
        if (tid == 0) __hip_atomic_store((GAS u32*)(ws + WS_F4) + (tt * 8 + ct) * 32, ep, __ATOMIC_RELAXED, __HIP_MEMORY_SCOPE_AGENT);
        TS(16);
        {
            const f32x2 st = *(const LAS f32x2*)(lds + G_PS + (32 + r32) * 8);
            const float ga = GC1 * st.x, gb = GC3 * st.x * st.x * st.x;
#pragma unroll
            for (int sI = 0; sI < 2; ++sI) {
                u32x4 v;
#pragma unroll
                for (int e = 0; e < 4; ++e) {
                    const int j = 8 * sI + 2 * e;
                    v[e] = pk_bf16(gelu_s(__builtin_fmaf(-st.y, cs[j], acc1[j]), st.x, ga, gb),
                                   gelu_s(__builtin_fmaf(-st.y, cs[j + 1], acc1[j + 1]), st.x, ga, gb));
                }
                __builtin_amdgcn_raw_buffer_store_b128(v, rs, gdst + 112 * 1024 + sI * 1024, 0, 16);
            }
        }
        TS(17);
        LBAR();
    }
    TS(18);
    {
        const u32 gsrc = WS_G + (tt * 2 * 112 + wave * 14) * 1024;
        const u32 ldst = lbase + wave * 14336;
#define FILL_A(t) do { dma_ws4(srd_ws, lane * 16, gsrc + (t) * 114688, ldst); dma_ws4(srd_ws, lane * 16, gsrc + (t) * 114688 + 4096, ldst + 4096); } while (0)
#define FILL_B(t, dst) do { dma_ws4(srd_ws, lane * 16, gsrc + (t) * 114688 + 8192, (dst)); dma_ws2(srd_ws, lane * 16, gsrc + (t) * 114688 + 12288, (dst) + 4096); } while (0)
        FILL_A(0); FILL_B(0, ldst + 8192);
        x1res = *(const LAS u32x4*)(lds + G_SPARE + wave * 2048 + lane * 16);
        hres = *(const LAS u32x4*)(lds + G_SPARE + wave * 2048 + 1024 + lane * 16);
        WAIT_V(14);
        LBAR();
        if (tid == 0) __hip_atomic_store((GAS u32*)(ws + WS_F5) + (tt * 8 + ct) * 32, ep, __ATOMIC_RELAXED, __HIP_MEMORY_SCOPE_AGENT);
        const LAS char* Gb = lds + kq * 28672 + lane * 16;
        const LAS char* Gs = lds + G_SPARE + kq * 12288 + lane * 16;
        f32x16 acc0 = {}, acc1 = {};
        u32x4 bq[4];
#define KA(i) ((i) < 8 ? (i) : (i) + 6)
#define KB(i) ((i) < 6 ? (i) + 8 : (i) + 16)
#define KS(i) (i)
#define PART(ACC, BASE, KF, KW, N) do { \
        _Pragma("unroll") for (int i_ = 0; i_ < 4; ++i_) bq[i_] = *(const LAS u32x4*)(BASE + KF(i_) * 1024); \
        _Pragma("unroll") for (int i_ = 0; i_ < N; ++i_) { \
            ACC = mfma32(w4f[KW(i_)], bq[i_ & 3], ACC); \
            if (i_ + 4 < N) bq[i_ & 3] = *(const LAS u32x4*)(BASE + KF(i_ + 4) * 1024); \
            SCHED0(); } } while (0)
        WAIT_V(6);
        LBAR();
        TS(20);
        PART(acc0, Gb, KA, KA, 16);
        if (wave == 7) POLL(f4p + 8192);
        WAIT_V(0);
#pragma unroll
        for (int i = 0; i < 12; ++i) PIN4(w4f[KB(i)]);
        LBAR();
        TS(21);
        FILL_A(1); FILL_B(1, lbase + G_SPARE + wave * 6144);
        PART(acc0, Gb, KB, KB, 12);
        WAIT_V(6);
        LBAR();
        TS(22);
        PART(acc1, Gb, KA, KA, 16);
        WAIT_V(0);
        LBAR();
        TS(23);
        PART(acc1, Gs, KS, KB, 12);
        TS(24);
#pragma unroll
        for (int p = 0; p < 4; ++p) {
            if (p != kq) {
                const int slot = kq > p ? kq - 1 : kq;
                LAS char* dst = lds + ((nq * 4 + p) * 3 + slot) * 2048 + lane * 16;
                const int o = 8 * (p & 1);
                if (p < 2) { *(LAS f32x4*)(dst) = f32x4{acc0[o], acc0[o + 1], acc0[o + 2], acc0[o + 3]};
                             *(LAS f32x4*)(dst + 1024) = f32x4{acc0[o + 4], acc0[o + 5], acc0[o + 6], acc0[o + 7]}; }
                else { *(LAS f32x4*)(dst) = f32x4{acc1[o], acc1[o + 1], acc1[o + 2], acc1[o + 3]};
                       *(LAS f32x4*)(dst + 1024) = f32x4{acc1[o + 4], acc1[o + 5], acc1[o + 6], acc1[o + 7]}; }
            }
        }
        float fin[8];
#pragma unroll
        for (int e = 0; e < 8; ++e) {
            float lo = tq ? acc1[e] : acc0[e], hi = tq ? acc1[8 + e] : acc0[8 + e];
            fin[e] = kh ? hi : lo;
        }
        PIN4(hres); PIN4(x1res);
        LBAR();
#pragma unroll
        for (int sl = 0; sl < 3; ++sl) {
            const LAS char* src = lds + ((nq * 4 + kq) * 3 + sl) * 2048 + lane * 16;
            f32x4 a0 = *(const LAS f32x4*)(src), a1 = *(const LAS f32x4*)(src + 1024);
#pragma unroll
            for (int e = 0; e < 4; ++e) { fin[e] += a0[e]; fin[4 + e] += a1[e]; }
        }
        u32 o[4];
#pragma unroll
        for (int e = 0; e < 4; ++e) {
            f32x2 h2 = unpk(pk_bf16(fin[2 * e], fin[2 * e + 1]));
            f32x2 t1 = unpk(pk_bf16v(unpk(x1res[e]) + h2));
            o[e] = pk_bf16v(unpk(hres[e]) + t1);
        }
        int tz = threadIdx.x;
        asm volatile("" : "+v"(tz));
        u16* orow = out + (size_t)(tt * 64 + tq * 32 + (tz & 31)) * D + ct * 64 + nq * 32 + kh * 16 + 4 * ((tz >> 5) & 1);
        *(u32x2*)(orow) = u32x2{o[0], o[1]};
        *(u32x2*)(orow + 8) = u32x2{o[2], o[3]};
    }
    TS(25);
}

extern "C" void kernel_launch(void* const* d_in, const int* in_sizes, int n_in, void* d_out, int out_size,
                              void* d_ws, size_t ws_size, hipStream_t stream) {
    (void)hipFuncSetAttribute((const void*)k_mega, hipFuncAttributeMaxDynamicSharedMemorySize, LDS_BYTES);
    k_mega<<<256, 512, LDS_BYTES, stream>>>((const u16*)d_in[0], (const u16*)d_in[1], (const u16*)d_in[2],
                                            (const u16*)d_in[3], (const u16*)d_in[4], (const u16*)d_in[5],
                                            (const u16*)d_in[6], (const u16*)d_in[7], (const u16*)d_in[8],
                                            (const u16*)d_in[10], (const u16*)d_in[11], (const u16*)d_in[12],
                                            (const u16*)d_in[13], (u16*)d_out, (char*)d_ws);
}
```

```cpp
#define CWARM
#define PTOUCH
#include <hip/hip_runtime.h>
#include <hip/hip_bf16.h>
#include <stdint.h>

#define DEV __device__ __forceinline__
#define LAS __attribute__((address_space(3)))
#define GAS __attribute__((address_space(1)))
typedef unsigned int u32;
typedef unsigned short u16;
typedef unsigned long long u64;
typedef float f32x2 __attribute__((ext_vector_type(2)));
typedef float f32x4 __attribute__((ext_vector_type(4)));
typedef float f32x16 __attribute__((ext_vector_type(16)));
typedef u32 u32x2 __attribute__((ext_vector_type(2)));
typedef u32 u32x4 __attribute__((ext_vector_type(4)));
typedef short s16x4 __attribute__((ext_vector_type(4)));
typedef __bf16 bf16x2 __attribute__((ext_vector_type(2)));
typedef __bf16 bf16x8 __attribute__((ext_vector_type(8)));
typedef _Float16 f16x2 __attribute__((ext_vector_type(2)));
typedef _Float16 f16x8 __attribute__((ext_vector_type(8)));

#define TS(i)

constexpr int L = 2048, D = 512, NS = 128, F = 1792;

constexpr u32 WS_CTL = 0;
constexpr u32 WS_F1 = 8192;
constexpr u32 WS_F0 = 16384;
constexpr u32 WS_F2 = 32768;
constexpr u32 WS_F3 = 65536;
constexpr u32 WS_F4 = 98304;
constexpr u32 WS_F5 = 131072;
constexpr u32 WS_WT3 = 163840;
constexpr u32 WS_WT4 = WS_WT3 + 512 * 1792 * 2;
constexpr u32 WS_HC = WS_WT4 + 512 * 1792 * 2;
constexpr u32 WS_HG = WS_HC + 2048 * 512 * 2;
constexpr u32 WS_X1 = WS_HG + 2048 * 512 * 2;
constexpr u32 WS_G = WS_X1 + 2048 * 512 * 2;
constexpr u32 WS_XU = WS_G + 2048 * 1792 * 2;
constexpr u32 XU_SLAB = 32768 + 128;
constexpr u32 WS_PST = WS_XU + 256 * XU_SLAB;
constexpr u32 WS_END = WS_PST + 64 * 8192;

__device__ const u32x4 g_zero16[4] = {};

constexpr int c_round_bf16(int Lg) {
    if (Lg < 256) return Lg;
    int e = 0;
    while ((Lg >> (e + 1)) != 0) ++e;
    int sh = e - 7, q = Lg >> sh, rem = Lg & ((1 << sh) - 1), half = 1 << (sh - 1);
    if (rem > half || (rem == half && (q & 1))) ++q;
    return q << sh;
}
constexpr int c_ci(int Lr) {
    if (Lr < 256) return Lr;
    int e = 0;
    while ((Lr >> (e + 1)) != 0) ++e;
    return 128 * (e - 6) + ((Lr >> (e - 7)) - 128);
}
constexpr int KC_ZERO = 644, KC_DUMMY = 648, KC_LEN = 656;
struct Tabs {
    u16 ci[3 * 64 * 4];
    u16 ex[256 * 12];
    constexpr Tabs() : ci(), ex() {
        for (int mt = 0; mt < 3; ++mt)
            for (int l = 0; l < 64; ++l)
                for (int e = 0; e < 4; ++e) {
                    int row = mt * 16 + (l >> 4) * 4 + e, j = l & 15, lrow = 0;
                    if (row < 16) lrow = (row & 7) + 128 * (row >> 3);
                    else if (row < 24) lrow = 256 + 2 * (row & 3) + 128 * ((row >> 2) & 1);
                    else if (row < 32) lrow = 512 + 4 * (row & 1) + 128 * ((row >> 1) & 3);
                    else if (row < 40) lrow = 1024 + 128 * (row & 7);
                    else lrow = 2048;
                    bool valid = row < 40 || (row == 40 && j == 0);
                    ci[(mt * 64 + l) * 4 + e] = (u16)(2 * (valid ? c_ci(lrow + 8 * j) : KC_DUMMY));
                }
        for (int c = 0; c < 256; ++c)
            for (int i = 0; i < 12; ++i) {
                int lag = 2047 - (8 * c + i);
                ex[c * 12 + i] = (u16)(2 * (lag < 0 ? KC_ZERO : c_ci(c_round_bf16(lag))));
            }
    }
};
__device__ const Tabs g_tabs = Tabs();

DEV float bf_lo(u32 v) { return __uint_as_float(v << 16); }
DEV float bf_hi(u32 v) { return __uint_as_float(v & 0xffff0000u); }
DEV float bf1(u16 v) { return __uint_as_float(((u32)v) << 16); }
DEV u32 pk_bf16(float a, float b) {
    f32x2 f = {a, b};
    bf16x2 r = __builtin_convertvector(f, bf16x2);
    return __builtin_bit_cast(u32, r);
}
DEV u32 pk_bf16v(f32x2 f) {
    bf16x2 r = __builtin_convertvector(f, bf16x2);
    return __builtin_bit_cast(u32, r);
}
DEV f32x2 unpk(u32 v) { return f32x2{bf_lo(v), bf_hi(v)}; }
DEV float rbf(float a) { return bf_lo(pk_bf16(a, 0.f)); }
DEV u32 pk_f16(float a, float b) {
    f32x2 f = {a, b};
    f16x2 r = __builtin_convertvector(f, f16x2);
    return __builtin_bit_cast(u32, r);
}
DEV float dot2bf(u32 a, u32 b, float c) {
    return __builtin_amdgcn_fdot2_f32_bf16(__builtin_bit_cast(bf16x2, a), __builtin_bit_cast(bf16x2, b), c, false);
}
DEV f32x16 mfma32(u32x4 a, u32x4 b, f32x16 c) {
    return __builtin_amdgcn_mfma_f32_32x32x16_bf16(__builtin_bit_cast(bf16x8, a), __builtin_bit_cast(bf16x8, b), c, 0, 0, 0);
}
DEV f32x4 mfma16h(u32x4 a, u32x4 b, f32x4 c) {
    return __builtin_amdgcn_mfma_f32_16x16x32_f16(__builtin_bit_cast(f16x8, a), __builtin_bit_cast(f16x8, b), c, 0, 0, 0);
}
DEV f32x2 gelu2(f32x2 x) {
    const float c1 = -2.0f * 0.7978845608028654f * 1.4426950408889634f;
    const float c3 = c1 * 0.044715f;
    f32x2 t = x * (x * x * c3 + c1);
    f32x2 e = {__builtin_amdgcn_exp2f(t.x), __builtin_amdgcn_exp2f(t.y)};
    e = e + 1.0f;
    f32x2 r = {__builtin_amdgcn_rcpf(e.x), __builtin_amdgcn_rcpf(e.y)};
    return x * r;
}
template <int CTRL, int RM>
DEV float dpp_f(float v) {
    return __int_as_float(__builtin_amdgcn_update_dpp(0, __float_as_int(v), CTRL, RM, 0xf, true));
}
DEV float wave_sum(float v) {
    v += dpp_f<0x111, 0xf>(v); v += dpp_f<0x112, 0xf>(v); v += dpp_f<0x114, 0xf>(v); v += dpp_f<0x118, 0xf>(v);
    v += dpp_f<0x142, 0xa>(v); v += dpp_f<0x143, 0xc>(v);
    return __int_as_float(__builtin_amdgcn_readlane(__float_as_int(v), 63));
}
DEV float wave_max(float v) {
    v = fmaxf(v, dpp_f<0x111, 0xf>(v)); v = fmaxf(v, dpp_f<0x112, 0xf>(v)); v = fmaxf(v, dpp_f<0x114, 0xf>(v));
    v = fmaxf(v, dpp_f<0x118, 0xf>(v)); v = fmaxf(v, dpp_f<0x142, 0xa>(v)); v = fmaxf(v, dpp_f<0x143, 0xc>(v));
    return __int_as_float(__builtin_amdgcn_readlane(__float_as_int(v), 63));
}
DEV float add_xor32(float v) {
    u32 u = __float_as_uint(v);
    auto sw = __builtin_amdgcn_permlane32_swap(u, u, false, false);
    u32 a = sw[0], b = sw[1];
    return __uint_as_float(a) + __uint_as_float(b);
}
#define WAIT_V(n) asm volatile("s_waitcnt vmcnt(" #n ")" ::: "memory")
#define FENCE() asm volatile("" ::: "memory")
#define LBAR() do { asm volatile("s_waitcnt lgkmcnt(0)" ::: "memory"); __builtin_amdgcn_s_barrier(); asm volatile("" ::: "memory"); } while (0)
#define PIN4(a) asm volatile("" : "+v"(a))

DEV void ln_consts(float mean, float var, float& m_bf, float& sd_bf) {
    m_bf = rbf(mean);
    float v = rbf(var);
    v = rbf(v + 1e-5f);
    sd_bf = rbf(__builtin_amdgcn_sqrtf(v));
}
DEV void dma_ws(u32x4 srd, u32 voff, u32 soff, u32 ldst) {
    asm volatile("s_mov_b32 m0, %0\n\ts_nop 0\n\tbuffer_load_dwordx4 %1, %2, %3 offen sc1 lds"
                 :: "s"(ldst), "v"(voff), "s"(srd), "s"(soff) : "memory");
}
DEV void dma_ws2(u32x4 srd, u32 voff, u32 soff, u32 ldst) {
    asm volatile("s_mov_b32 m0, %0\n\ts_nop 0\n\tbuffer_load_dwordx4 %1, %2, %3 offen sc1 lds\n\t"
                 "buffer_load_dwordx4 %1, %2, %3 offen offset:1024 sc1 lds"
                 :: "s"(ldst), "v"(voff), "s"(srd), "s"(soff) : "memory");
}
DEV void dma_ws4(u32x4 srd, u32 voff, u32 soff, u32 ldst) {
    asm volatile("s_mov_b32 m0, %0\n\ts_nop 0\n\tbuffer_load_dwordx4 %1, %2, %3 offen sc1 lds\n\t"
                 "buffer_load_dwordx4 %1, %2, %3 offen offset:1024 sc1 lds\n\t"
                 "buffer_load_dwordx4 %1, %2, %3 offen offset:2048 sc1 lds\n\t"
                 "buffer_load_dwordx4 %1, %2, %3 offen offset:3072 sc1 lds"
                 :: "s"(ldst), "v"(voff), "s"(srd), "s"(soff) : "memory");
}
DEV void dma_in(u32x4 srd, u32 voff, u32 soff, u32 ldst) {
    asm volatile("s_mov_b32 m0, %0\n\ts_nop 0\n\tbuffer_load_dwordx4 %1, %2, %3 offen lds"
                 :: "s"(ldst), "v"(voff), "s"(srd), "s"(soff) : "memory");
}
DEV void dma_g(const void* g, u32 ldst) {
    asm volatile("s_mov_b32 m0, %0\n\ts_nop 0\n\tglobal_load_lds_dwordx4 %1, off sc1"
                 :: "s"(ldst), "v"(g) : "memory");
}
DEV u32x4 make_srd(const void* p, u32 bytes) {
    u64 a = (u64)p;
    return u32x4{(u32)a, (u32)(a >> 32) & 0xffffu, bytes, 0x00020000u};
}
#define SCHED0() __builtin_amdgcn_sched_barrier(0)
DEV u32x4 tr_frag(const LAS char* p) {
    s16x4 a = __builtin_amdgcn_ds_read_tr16_b64_v4i16((LAS s16x4*)p);
    s16x4 b = __builtin_amdgcn_ds_read_tr16_b64_v4i16((LAS s16x4*)(p + 1024));
    u32x2 x = __builtin_bit_cast(u32x2, a), y = __builtin_bit_cast(u32x2, b);
    return u32x4{x[0], x[1], y[0], y[1]};
}

struct cplx { float x, y; };
DEV cplx cmul(cplx a, cplx b) {
    cplx r;
    asm("v_mul_f32 %0, %3, %5\n\tv_mul_f32 %1, %3, %4\n\tv_fma_f32 %0, %2, %4, -%0\n\tv_fma_f32 %1, %2, %5, %1"
        : "=&v"(r.x), "=&v"(r.y) : "v"(a.x), "v"(a.y), "v"(b.x), "v"(b.y));
    return r;
}
DEV cplx csq(cplx a) {
    cplx r;
    asm("v_mul_f32 %0, %3, %3\n\tv_mul_f32 %1, %2, %3\n\tv_fma_f32 %0, %2, %2, -%0\n\tv_add_f32 %1, %1, %1"
        : "=&v"(r.x), "=&v"(r.y) : "v"(a.x), "v"(a.y));
    return r;
}
DEV cplx cexp_pow2(float tr, float wh, float wl, float p2) {
    float mag = __builtin_amdgcn_exp2f(tr * p2);
    float ph = __builtin_amdgcn_fractf(wh * p2) + wl * p2;
    return cplx{mag * __builtin_amdgcn_cosf(ph), mag * __builtin_amdgcn_sinf(ph)};
}
template <int CTRL, int RM>
DEV u32 dpp_u(u32 v) { return (u32)__builtin_amdgcn_update_dpp(0, (int)v, CTRL, RM, 0xf, true); }
DEV u32 wave_max_u(u32 v) {
    v = max(v, dpp_u<0x111, 0xf>(v)); v = max(v, dpp_u<0x112, 0xf>(v)); v = max(v, dpp_u<0x114, 0xf>(v));
    v = max(v, dpp_u<0x118, 0xf>(v)); v = max(v, dpp_u<0x142, 0xa>(v)); v = max(v, dpp_u<0x143, 0xc>(v));
    return (u32)__builtin_amdgcn_readlane((int)v, 63);
}
DEV float gelu_s(float d, float r, float ga, float gb) {
    float t = d * __builtin_fmaf(d * d, gb, ga);
    float e = __builtin_amdgcn_exp2f(t);
    return (d * r) * __builtin_amdgcn_rcpf(1.0f + e);
}
constexpr float GC1 = -2.0f * 0.7978845608028654f * 1.4426950408889634f, GC3 = GC1 * 0.044715f;

constexpr int TRS = 528;
constexpr int RCOPY = 4160;
constexpr int A_R = 0;
constexpr int A_U = 33280, UPAD = 240, ULEN = 2304;
constexpr int A_KC = A_U + 2 * ULEN * 2;
constexpr int A_X = 45312, A_XCH = 41 * TRS;
constexpr int A_Y = A_X + 2 * A_XCH, A_YCH = 16 * TRS;
constexpr int A_SCR = 107520;
constexpr int A_YP = 45312;
constexpr int A_W = 98304;
constexpr int A_XI = 123904, XI_RS = 1088;
constexpr int LDS_BYTES = 163840;
constexpr int G_ACT = 0, G_RED = 65536, G_PS = 81920, G_CS = 86016, G_SPARE = 114688;
constexpr int SPIN_MAX = 1 << 18;

__global__ __launch_bounds__(512, 2) void k_mega(const u16* __restrict__ x, const u16* __restrict__ A_re,
                                                 const u16* __restrict__ A_im, const u16* __restrict__ B_re,
                                                 const u16* __restrict__ B_im, const u16* __restrict__ C_re,
                                                 const u16* __restrict__ C_im, const u16* __restrict__ log_dt,
                                                 const u16* __restrict__ Dskip, const u16* __restrict__ w1,
                                                 const u16* __restrict__ w2, const u16* __restrict__ w3,
                                                 const u16* __restrict__ w4, u16* __restrict__ out, char* ws) {
    extern __shared__ __attribute__((aligned(16))) char lds_raw[];
    LAS char* lds = (LAS char*)lds_raw;
    const int tid = threadIdx.x, lane = tid & 63, wave = __builtin_amdgcn_readfirstlane(tid >> 6);
    const int b = blockIdx.x, bq = b >> 3;
    const int cb = (b & 7) * 32 + bq, d0 = cb * 2;
    const int tt = (b & 7) * 4 + (bq >> 3), ct = bq & 7;
    const auto rs = __builtin_amdgcn_make_buffer_rsrc(ws, 0, WS_END, 0x00020000);
    const u32x4 srd_ws = make_srd(ws, WS_END), srd_w1 = make_srd(w1, D * D * 2), srd_w2 = make_srd(w2, D * D * 2);
    const u32 lbase = (u32)(size_t)lds;
    asm volatile("" :: "s"(x), "s"(A_re), "s"(A_im), "s"(B_re), "s"(B_im), "s"(C_re), "s"(C_im), "s"(log_dt), "s"(Dskip),
                 "s"(w1), "s"(w2), "s"(w3), "s"(w4), "s"(out), "s"(ws));
    u32 cw0 = 0;
    {
        u64 pc;
        asm volatile("s_getpc_b64 %0" : "=s"(pc));
        const char* cp = (const char*)(pc & ~127ull) + ((bq & 15) + 16 * lane) * 128;
        if (wave == 0 && lane < 24) asm volatile("global_load_dword %0, %1, off" : "=v"(cw0) : "v"(cp) : "memory");
    }
    u32 pt0 = 0, pt1 = 0;
    if (wave == 1) {
        const char* pa = lane < 16 ? (const char*)ws + (size_t)(((bq & 31) * 16 + lane) * 65536u) % WS_END
                       : (lane < 32 ? (const char*)out + (size_t)((lane - 16) * 131072u + (bq & 1) * 65536u)
                                    : (const char*)(lane < 48 ? w1 : w2) + (size_t)((lane & 15) * 32768u));
        asm volatile("global_load_dword %0, %1, off" : "=v"(pt0) : "v"(pa) : "memory");
    }
    TS(0);

    const u32 ep = *(const GAS u32*)(ws + WS_CTL) + 1u;
    u32x4 xr = *(const u32x4*)(x + (size_t)(b * 8 + wave) * D + lane * 8);
    const int kch = wave >> 2, knh = wave & 1, kpart = (wave >> 1) & 1;
    const int pidx = (d0 + kch) * NS + knh * 64 + lane;
    u16 p_are = A_re[pidx], p_aim = A_im[pidx], p_bre = B_re[pidx], p_bim = B_im[pidx], p_cre = C_re[pidx], p_cim = C_im[pidx];
    u16 o_bre = B_re[pidx ^ 64], o_bim = B_im[pidx ^ 64], o_cre = C_re[pidx ^ 64], o_cim = C_im[pidx ^ 64];
    u32 p_ldt = *(const u32*)(log_dt + d0);
    u32 p_dsk = *(const u32*)(Dskip + d0);
    u32x4 traw[2];
    u32 tdst = 0;
    {
        int pr = b * 7 + (wave < 7 ? wave : 0);
        const u16* src; int N;
        if (pr < 896) { int ntp = pr >> 5, ks = pr & 31; src = w3 + (size_t)(16 * ks) * F + ntp * 64; N = F;
                        tdst = WS_WT3 + ((ntp * 2) * 32 + ks) * 1024; }
        else { int p2 = pr - 896; int ntp = p2 / 112, ks = p2 - ntp * 112; src = w4 + (size_t)(16 * ks) * D + ntp * 64; N = D;
               tdst = WS_WT4 + ((ntp * 2) * 112 + ks) * 1024; }
#pragma unroll
        for (int i = 0; i < 2; ++i) traw[i] = *(const u32x4*)(src + (size_t)(8 * i + (lane >> 3)) * N + (lane & 7) * 8);
    }
    u32x2 tci = *(const u32x2*)(g_tabs.ci + ((wave & 3) < 3 ? (wave & 3) : 0) * 256 + lane * 4);
    u32x4 tex0 = *(const u32x4*)(g_tabs.ex + (tid & 255) * 12);
    u32x2 tex1 = *(const u32x2*)(g_tabs.ex + (tid & 255) * 12 + 8);

    const int trq = (lane & 15) >> 2, trp = lane & 3, trh = lane >> 5, trg = (lane >> 4) & 1;
    const int tr_off = (4 * trh + trq) * 128 + (((2 * trg + (trp >> 1)) ^ ((trq >> 1) << 2)) << 4) + 8 * (trp & 1);
    const int dma_sw = ((lane & 7) ^ (((lane >> 4) & 1) << 2)) * 8;
    {
        f32x2 f[4];
#pragma unroll
        for (int i = 0; i < 4; ++i) f[i] = unpk(xr[i]);
        f32x2 s2 = (f[0] + f[1]) + (f[2] + f[3]);
        float mean = wave_sum(s2.x + s2.y) * (1.0f / D);
        f32x2 q2 = {0.f, 0.f};
#pragma unroll
        for (int i = 0; i < 4; ++i) { f32x2 dd = f[i] - mean; q2 += dd * dd; }
        float var = wave_sum(q2.x + q2.y) * (1.0f / D);
        float m, sd;
        ln_consts(mean, var, m, sd);
        const float rsd = __builtin_amdgcn_rcpf(sd);
        u32x4 uo;
#pragma unroll
        for (int i = 0; i < 4; ++i) {
            f32x2 dl = unpk(pk_bf16v(f[i] - m));
            f32x2 u = unpk(pk_bf16v(dl * rsd));
            uo[i] = pk_f16(u.x, u.y);
        }
        {
            LAS char* gi = lds + A_XI + lane * 264 + wave * 4;
#pragma unroll
            for (int i = 0; i < 4; ++i) {
                *(LAS u32*)(gi + (2 * i) * 32) = __builtin_amdgcn_perm(uo[i], xr[i], 0x05040100);
                *(LAS u32*)(gi + (2 * i + 1) * 32) = __builtin_amdgcn_perm(uo[i], xr[i], 0x07060302);
            }
        }
        LAS char* scr = lds + A_SCR + wave * 2048;
#pragma unroll
        for (int i = 0; i < 2; ++i)
            *(LAS u32x4*)(scr + i * 1024 + (lane >> 3) * 128 + (((lane & 7) ^ (((lane >> 4) & 1) << 2)) << 4)) = traw[i];
        u32x4 f0 = tr_frag(scr + tr_off), f1 = tr_frag(scr + (tr_off ^ 64));
        if (wave < 7) {
            const u32 nstride = tdst < WS_WT4 ? 32 * 1024 : 112 * 1024;
            __builtin_amdgcn_raw_buffer_store_b128(f0, rs, tdst + lane * 16, 0, 16);
            __builtin_amdgcn_raw_buffer_store_b128(f1, rs, tdst + nstride + lane * 16, 0, 16);
        }
        LBAR();
        const LAS char* gp = lds + A_XI + (wave * 8 + (lane >> 5)) * 264 + ((lane >> 2) & 7) * 32 + (lane & 3) * 8;
#pragma unroll
        for (int k = 0; k < 4; ++k) {
            u32x2 v = *(const LAS u32x2*)(gp + k * 528);
            __builtin_amdgcn_raw_buffer_store_b128(u32x4{v[0], ep, v[1], ep}, rs, WS_XU + b * XU_SLAB + wave * 4096 + k * 1024 + lane * 16, 0, 16);
        }
        FENCE();
    }
    TS(1);
    float inv_scale[2];
    {
        float dts[2] = {rbf(__expf(bf_lo(p_ldt))), rbf(__expf(bf_hi(p_ldt)))};
        float dt = kch ? dts[1] : dts[0];
        float zr = dt * bf1(p_are), zi = dt * bf1(p_aim);
        float tr = zr * 1.4426950408889634f;
        const float inv2pi_h = 0.15915494309189535f;
        const float inv2pi_l = (float)(0.15915494309189533576888 - (double)inv2pi_h);
        float wh = zi * inv2pi_h;
        float wl = __builtin_fmaf(zi, inv2pi_h, -wh) + zi * inv2pi_l;
        cplx CB = cmul(cplx{bf1(p_cre), bf1(p_cim)}, cplx{dt * bf1(p_bre), dt * bf1(p_bim)});
        cplx CO = cmul(cplx{bf1(o_cre), bf1(o_cim)}, cplx{dt * bf1(o_bre), dt * bf1(o_bim)});
        u32 mx = wave_max_u(max(max(__float_as_uint(CB.x) & 0x7fffffffu, __float_as_uint(CB.y) & 0x7fffffffu),
                                max(__float_as_uint(CO.x) & 0x7fffffffu, __float_as_uint(CO.y) & 0x7fffffffu)));
        int e = (int)(mx >> 23);
        e = e < 8 ? 8 : (e > 246 ? 246 : e);
        float sc = __uint_as_float((u32)(254 + 6 - e) << 23);
        CB.x *= sc; CB.y *= sc;
        if (lane == 0 && knh == 0 && kpart == 0) {
            *(LAS float*)(lds + A_KC + (kch * KC_LEN + 652) * 2) = __uint_as_float((u32)(e - 6) << 23);
            *(LAS u32*)(lds + A_KC + (kch * KC_LEN + KC_ZERO) * 2) = 0u;
        }
        LAS char* Xb = lds + A_X + kch * A_XCH + (knh * 64 + lane) * 4;
        LAS char* Yb = lds + A_Y + kch * A_YCH + (knh * 64 + lane) * 4;
#define PUTX(row, v) *(LAS u32*)(Xb + (row) * TRS) = pk_f16((v).x, (v).y)
#define PUTY(row, v) *(LAS u32*)(Yb + (row) * TRS) = pk_f16((v).x, (v).y)
        if (kpart == 0) {
            cplx P1 = cexp_pow2(tr, wh, wl, 1.f), Q8 = cexp_pow2(tr, -wh, -wl, 8.f), P128 = cexp_pow2(tr, wh, wl, 128.f);
            cplx P2 = csq(P1), Q16 = csq(Q8), P4 = csq(P2), Q32 = csq(Q16), Q64 = csq(Q32);
            cplx Y[8];
            Y[0] = cplx{1.f, 0.f}; Y[1] = Q8; Y[2] = Q16; Y[3] = cmul(Q8, Q16); Y[4] = Q32;
            Y[5] = cmul(Q32, Q8); Y[6] = cmul(Q32, Q16); Y[7] = cmul(Q32, Y[3]);
#pragma unroll
            for (int j = 0; j < 8; ++j) PUTY(j, Y[j]);
            PUTY(8, Q64);
#pragma unroll
            for (int j = 1; j < 8; ++j) { cplx v = cmul(Q64, Y[j]); PUTY(8 + j, v); }
            cplx Xa[8];
            Xa[0] = CB; Xa[1] = cmul(CB, P1); Xa[2] = cmul(CB, P2); Xa[3] = cmul(Xa[2], P1);
#pragma unroll
            for (int j = 0; j < 4; ++j) Xa[4 + j] = cmul(Xa[j], P4);
#pragma unroll
            for (int j = 0; j < 8; ++j) { PUTX(j, Xa[j]); cplx v = cmul(Xa[j], P128); PUTX(8 + j, v); }
        } else {
            cplx P2 = cexp_pow2(tr, wh, wl, 2.f), P128 = cexp_pow2(tr, wh, wl, 128.f), P1024 = cexp_pow2(tr, wh, wl, 1024.f);
            cplx P4 = csq(P2), P256 = csq(P128), P2048 = csq(P1024), P512 = csq(P256);
            cplx X0 = CB, X2 = cmul(CB, P2), X4 = cmul(CB, P4), X6 = cmul(X2, P4);
            cplx X128 = cmul(CB, P128), X132 = cmul(X4, P128);
            cplx c0[4] = {cmul(X0, P256), cmul(X2, P256), cmul(X4, P256), cmul(X6, P256)};
            cplx c1[4];
#pragma unroll
            for (int j = 0; j < 4; ++j) c1[j] = cmul(c0[j], P128);
#pragma unroll
            for (int j = 0; j < 4; ++j) { PUTX(16 + j, c0[j]); PUTX(20 + j, c1[j]); }
            cplx dd[8] = {cmul(X0, P512), cmul(X4, P512), cmul(X128, P512), cmul(X132, P512),
                          cmul(c0[0], P512), cmul(c0[2], P512), cmul(c1[0], P512), cmul(c1[2], P512)};
#pragma unroll
            for (int j = 0; j < 8; ++j) PUTX(24 + j, dd[j]);
            cplx ee[8] = {X0, X128, c0[0], c1[0], dd[0], dd[2], dd[4], dd[6]};
#pragma unroll
            for (int j = 0; j < 8; ++j) { cplx v = cmul(ee[j], P1024); PUTX(32 + j, v); }
            cplx v = cmul(CB, P2048);
            PUTX(40, v);
        }
    }

    WAIT_V(4);
    LBAR();
    if (tid < 8) __hip_atomic_store((GAS u32*)(ws + WS_F0) + tid * 256 + b, ep, __ATOMIC_RELAXED, __HIP_MEMORY_SCOPE_AGENT);
    asm volatile("" :: "v"(cw0));
    asm volatile("" :: "v"(pt0), "v"(pt1));
    TS(2);
    if ((wave & 3) < 3) {
        const int mt = wave & 3, r = lane & 15, g = lane >> 4;
        const LAS char* Xb = lds + A_X + kch * A_XCH + (mt * 16 + r) * TRS + g * 16;
        const LAS char* Yb = lds + A_Y + kch * A_YCH + r * TRS + g * 16;
        f32x4 acc0 = {0, 0, 0, 0}, acc1 = {0, 0, 0, 0};
#pragma unroll
        for (int ks = 0; ks < 8; ks += 2) {
            acc0 = mfma16h(*(const LAS u32x4*)(Xb + ks * 64), *(const LAS u32x4*)(Yb + ks * 64), acc0);
            acc1 = mfma16h(*(const LAS u32x4*)(Xb + ks * 64 + 64), *(const LAS u32x4*)(Yb + ks * 64 + 64), acc1);
        }
        acc0 += acc1;
        u32 k01 = pk_f16(acc0[0], acc0[1]), k23 = pk_f16(acc0[2], acc0[3]);
        LAS char* Kc = lds + A_KC + kch * KC_LEN * 2;
        *(LAS u16*)(Kc + (tci[0] & 0xffff)) = (u16)k01;
        *(LAS u16*)(Kc + (tci[0] >> 16)) = (u16)(k01 >> 16);
        *(LAS u16*)(Kc + (tci[1] & 0xffff)) = (u16)k23;
        *(LAS u16*)(Kc + (tci[1] >> 16)) = (u16)(k23 >> 16);
    }
    const u32 xuoff = WS_XU + (tid >> 1) * XU_SLAB + (d0 + (tid & 1)) * 64;
    u32x4 gq[4];
#pragma unroll
    for (int k = 0; k < 4; ++k) gq[k] = __builtin_amdgcn_raw_buffer_load_b128(rs, xuoff + k * 16, 0, 16);
    FENCE();
    LBAR();
    TS(3);
    {
        const int ch = tid >> 8, c = tid & 255;
        const LAS char* Kc = lds + A_KC + ch * KC_LEN * 2;
        u32 pe[6];
        u32 tw[6] = {tex0[0], tex0[1], tex0[2], tex0[3], tex1[0], tex1[1]};
#pragma unroll
        for (int i = 0; i < 6; ++i) {
            u32 lo = *(const LAS u16*)(Kc + (tw[i] & 0xffff));
            u32 hi = *(const LAS u16*)(Kc + (tw[i] >> 16));
            pe[i] = lo | (hi << 16);
        }
        u32 po[5];
#pragma unroll
        for (int i = 0; i < 5; ++i) po[i] = __builtin_amdgcn_alignbit(pe[i + 1], pe[i], 16);
        inv_scale[0] = *(const LAS float*)(lds + A_KC + 652 * 2);
        inv_scale[1] = *(const LAS float*)(lds + A_KC + (KC_LEN + 652) * 2);
        LAS char* Rb = lds + A_R + ch * 4 * RCOPY + c * 16;
        *(LAS u32x4*)(Rb + 0 * RCOPY) = u32x4{pe[0], pe[1], pe[2], pe[3]};
        *(LAS u32x4*)(Rb + 1 * RCOPY) = u32x4{po[0], po[1], po[2], po[3]};
        *(LAS u32x4*)(Rb + 2 * RCOPY) = u32x4{pe[1], pe[2], pe[3], pe[4]};
        *(LAS u32x4*)(Rb + 3 * RCOPY) = u32x4{po[1], po[2], po[3], po[4]};
        if (tid < 16) *(LAS u32x4*)(lds + A_R + (tid >> 1) * RCOPY + 4096 + (tid & 1) * 16) = u32x4{0, 0, 0, 0};
        if (tid >= 64 && tid < 124) {
            *(LAS u32x2*)(lds + A_U + (tid - 64) * 8) = u32x2{0, 0};
            *(LAS u32x2*)(lds + A_U + ULEN * 2 + (tid - 64) * 8) = u32x2{0, 0};
        }
    }

    _Pragma("unroll 1") for (int spin = 0; spin < SPIN_MAX; ++spin) {
        bool ok = true;
#pragma unroll
        for (int k = 0; k < 4; ++k) ok = ok && gq[k][1] == ep && gq[k][3] == ep;
        if (__all(ok)) break;
        __builtin_amdgcn_s_sleep(2);
        FENCE();
#pragma unroll
        for (int k = 0; k < 4; ++k) gq[k] = __builtin_amdgcn_raw_buffer_load_b128(rs, xuoff + k * 16, 0, 16);
    }
    u32x4 xq, uq;
#pragma unroll
    for (int k = 0; k < 4; ++k) {
        xq[k] = __builtin_amdgcn_perm(gq[k][2], gq[k][0], 0x05040100);
        uq[k] = __builtin_amdgcn_perm(gq[k][2], gq[k][0], 0x07060302);
    }
    *(LAS u32x4*)(lds + A_U + (tid & 1) * ULEN * 2 + (UPAD + (tid >> 1) * 8) * 2) = uq;
    if (wave == 7) {
        _Pragma("unroll 1") for (int spin = 0; spin < SPIN_MAX; ++spin) {
            FENCE();
            u32x4 f = __builtin_amdgcn_raw_buffer_load_b128(rs, WS_F0 + (b & 7) * 1024 + lane * 16, 0, 16);
            if (__all(f[0] == ep && f[1] == ep && f[2] == ep && f[3] == ep)) break;
            __builtin_amdgcn_s_sleep(2);
        }
    }
    const u32 w3off = wave < 7 ? WS_WT3 + (ct * 7 + wave) * 32768 + lane * 16 : 0xF0000000u;
    u32x4 w3f[32];
    float c3a = 0.f, c3b = 0.f;
#define CS3(lo, hi) do { _Pragma("unroll") for (int i_ = (lo); i_ < (hi); ++i_) { \
        c3a = dot2bf(w3f[i_][0], 0x3f803f80u, c3a); c3b = dot2bf(w3f[i_][1], 0x3f803f80u, c3b); \
        c3a = dot2bf(w3f[i_][2], 0x3f803f80u, c3a); c3b = dot2bf(w3f[i_][3], 0x3f803f80u, c3b); } } while (0)
#define W3L1(i) w3f[i] = __builtin_amdgcn_raw_buffer_load_b128(rs, w3off + (i) * 1024, 0, 16)
#define W3LOAD(lo, hi) do { FENCE(); _Pragma("unroll") for (int i_ = (lo); i_ < (hi); ++i_) \
        w3f[i_] = __builtin_amdgcn_raw_buffer_load_b128(rs, w3off + i_ * 1024, 0, 16); FENCE(); } while (0)
    const u32 img_voff = (lane >> 3) * (D * 2) + ct * 128 + dma_sw * 2;
    LBAR();
    TS(4);
    {
        const int ch = wave >> 2, rq = wave & 3;
        const int r = lane & 15, g = lane >> 4;
        const int o0 = 15 - r + 8 * g;
        const LAS char* Ab = lds + A_R + ch * 4 * RCOPY + (o0 & 3) * RCOPY + (o0 & ~3) * 2 + (6 - rq * 2) * 64;
        const LAS char* Bb = lds + A_U + ch * ULEN * 2 + (16 * r + 8 * g) * 2 + (6 - rq * 2) * 64;
        f32x4 acc[8];
#pragma unroll
        for (int i = 0; i < 8; ++i) acc[i] = f32x4{0, 0, 0, 0};
        u32x4 A0[8], B0[8], A1[8], B1[8];
#define LD1(AF, BF, a, rr) do { \
            u32x2 lo_ = *(const volatile LAS u32x2*)(Ab + (1 - (rr)) * 64 + (7 - (a)) * 512); \
            u32x2 hi_ = *(const volatile LAS u32x2*)(Ab + (1 - (rr)) * 64 + (7 - (a)) * 512 + 8); \
            AF[a] = u32x4{lo_[0], lo_[1], hi_[0], hi_[1]}; \
            BF[a] = *(const LAS u32x4*)(Bb + (1 - (rr)) * 64 + (a) * 512); } while (0)
#define GRP(AF, BF, m) do { _Pragma("unroll") for (int o_ = 0; o_ <= (m); ++o_) if (o_ + (m) <= 7) { \
            acc[o_ + (m)] = mfma16h(AF[m], BF[o_], acc[o_ + (m)]); \
            if (o_ != (m)) acc[o_ + (m)] = mfma16h(AF[o_], BF[m], acc[o_ + (m)]); } } while (0)
        LD1(A0, B0, 0, 0); LD1(A0, B0, 7, 0); LD1(A0, B0, 1, 0); LD1(A0, B0, 6, 0);
        SCHED0();
        GRP(A0, B0, 7); LD1(A0, B0, 2, 0); LD1(A0, B0, 5, 0); SCHED0();
        GRP(A0, B0, 6); LD1(A0, B0, 3, 0); LD1(A0, B0, 4, 0); W3L1(0); SCHED0();
        GRP(A0, B0, 5); W3L1(1); W3L1(2); SCHED0();
        GRP(A0, B0, 4); LD1(A1, B1, 0, 1); W3L1(3); W3L1(4); SCHED0();
        GRP(A0, B0, 3); LD1(A1, B1, 1, 1); W3L1(5); W3L1(6); SCHED0();
        GRP(A0, B0, 2); LD1(A1, B1, 2, 1); W3L1(7); SCHED0();
        GRP(A0, B0, 1); LD1(A1, B1, 3, 1); W3L1(8); SCHED0();
        GRP(A0, B0, 0); GRP(A1, B1, 0); LD1(A1, B1, 4, 1); W3L1(9); SCHED0();
        GRP(A1, B1, 1); LD1(A1, B1, 5, 1); W3L1(10); SCHED0();
        GRP(A1, B1, 2); LD1(A1, B1, 6, 1); W3L1(11); W3L1(12); SCHED0();
        GRP(A1, B1, 3); LD1(A1, B1, 7, 1); W3L1(13); W3L1(14); SCHED0();
        GRP(A1, B1, 4); W3L1(15); W3L1(16); SCHED0();
        GRP(A1, B1, 5); W3L1(17); W3L1(18); SCHED0();
        GRP(A1, B1, 6); W3L1(19); SCHED0();
        GRP(A1, B1, 7);
        LAS char* Yp = lds + A_YP + (rq * 2 + ch) * 8192 + g * 256 + ((r ^ (4 * g) ^ ch) << 4);
#pragma unroll
        for (int c = 0; c < 8; ++c) *(LAS f32x4*)(Yp + 1024 * c) = acc[c];
    }
    LBAR();
    TS(5);
    {
        const int ch = lane & 1, g0 = lane & 2, ii = lane >> 2;
        const LAS char* Yp = lds + A_YP + ch * 8192 + wave * 1024;
        const int oa = g0 * 256 + ((ii ^ (4 * g0) ^ ch) << 4), ob = (g0 + 1) * 256 + ((ii ^ (4 * g0 + 4) ^ ch) << 4);
        f32x4 ya = (*(const LAS f32x4*)(Yp + oa) + *(const LAS f32x4*)(Yp + 16384 + oa)) +
                   (*(const LAS f32x4*)(Yp + 32768 + oa) + *(const LAS f32x4*)(Yp + 49152 + oa));
        f32x4 yb = (*(const LAS f32x4*)(Yp + ob) + *(const LAS f32x4*)(Yp + 16384 + ob)) +
                   (*(const LAS f32x4*)(Yp + 32768 + ob) + *(const LAS f32x4*)(Yp + 49152 + ob));
        const float isc = ch ? inv_scale[1] : inv_scale[0];
        const float dsk = ch ? bf_hi(p_dsk) : bf_lo(p_dsk);
        u32x4 o;
#pragma unroll
        for (int k = 0; k < 4; ++k) {
            const float y0 = k < 2 ? ya[2 * k] : yb[2 * k - 4], y1 = k < 2 ? ya[2 * k + 1] : yb[2 * k - 3];
            f32x2 yy = unpk(pk_bf16(y0 * isc, y1 * isc));
            f16x2 uh = __builtin_bit_cast(f16x2, (u32)uq[k]);
            float ul = (float)uh.x, uhh = (float)uh.y;
            f32x2 ud = unpk(pk_bf16(ul * dsk, uhh * dsk));
            f32x2 a = unpk(pk_bf16v(yy + ud));
            o[k] = pk_bf16v(unpk(xq[k]) + a);
        }
        __builtin_amdgcn_raw_buffer_store_b128(o, rs, WS_HC + (tid >> 4) * 65536 + (d0 + ch) * 128 + ((tid >> 1) & 7) * 16, 0, 16);
    }
    LBAR();
#pragma unroll
    for (int i = 0; i < 8; ++i) dma_in(srd_w1, img_voff, (wave * 8 + i) * 8 * D * 2, lbase + G_ACT + (wave * 8 + i) * 1024);
    WAIT_V(8);
    LBAR();
    if (tid < 8) __hip_atomic_store((GAS u32*)(ws + WS_F1) + tid * 256 + b, ep, __ATOMIC_RELAXED, __HIP_MEMORY_SCOPE_AGENT);
    TS(6);
#pragma unroll
    for (int i = 0; i < 8; ++i) dma_in(srd_w2, img_voff, (wave * 8 + i) * 8 * D * 2, lbase + A_W + (wave * 8 + i) * 1024);
    WAIT_V(8);
    LBAR();

    const int tq = wave & 1, nq = (wave >> 1) & 1, kh = wave >> 2;
    const int r32 = lane & 31, h32 = lane >> 5;
    const u32 ones = 0x3f803f80u;
    u32x4 wf[32];
    {
        const LAS char* WbE = lds + G_ACT + kh * 8192 + (tr_off ^ (nq << 6)) + nq * 2048;
        const LAS char* WbO = WbE - nq * 4096;
#pragma unroll
        for (int i = 0; i < 16; ++i) wf[i] = tr_frag(((i & 1) ? WbO : WbE) + ((i >> 2) * 8 + (i & 3)) * 2048);
        float ca = 0.f, cb_ = 0.f;
#pragma unroll
        for (int i = 0; i < 16; ++i) {
            ca = dot2bf(wf[i][0], ones, ca); cb_ = dot2bf(wf[i][1], ones, cb_);
            ca = dot2bf(wf[i][2], ones, ca); cb_ = dot2bf(wf[i][3], ones, cb_);
        }
        const float csum = add_xor32(ca + cb_);
        if (h32 == 0 && tq == 0) *(LAS float*)(lds + G_CS + ((nq * 2 + kh) * 32 + r32) * 4) = csum;
    }
    if (wave < 7) CS3(0, 20);
    if (wave == 7) {
        _Pragma("unroll 1") for (int spin = 0; spin < SPIN_MAX; ++spin) {
            FENCE();
            u32x4 f = __builtin_amdgcn_raw_buffer_load_b128(rs, WS_F1 + (b & 7) * 1024 + lane * 16, 0, 16);
            if (__all(f[0] == ep && f[1] == ep && f[2] == ep && f[3] == ep)) break;
            __builtin_amdgcn_s_sleep(1);
        }
        if (b == 0 && lane == 0) *(GAS u32*)(ws + WS_CTL) = ep;
    }
    LBAR();
    TS(7);
    {
        const u32 voff = (lane >> 3) * 128 + dma_sw * 2;
#pragma unroll
        for (int c = 0; c < 4; ++c)
            dma_ws2(srd_ws, voff, WS_HC + tt * 65536 + (8 * c + wave) * 2048, lbase + G_ACT + (8 * c + wave) * 2048);
    }
    float csv[8];
    {
        const LAS char* p = lds + G_CS + nq * 256 + (16 * kh + 4 * h32) * 4;
        f32x4 a0 = *(const LAS f32x4*)(p), a1 = *(const LAS f32x4*)(p + 32);
        f32x4 b0 = *(const LAS f32x4*)(p + 128), b1 = *(const LAS f32x4*)(p + 160);
#pragma unroll
        for (int e = 0; e < 4; ++e) { csv[e] = a0[e] + b0[e]; csv[4 + e] = a1[e] + b1[e]; }
    }
    u32x4 hres, x1res;
    {
        f32x16 acc = {};
        u32x4 own[2];
        float s1a = 0.f, s1b = 0.f, q1a = 0.f, q1b = 0.f;
        const LAS char* Ab = lds + G_ACT + (tr_off ^ (tq << 6));
        const LAS char* AbE = Ab + kh * 8192 + nq * 2048;
        const LAS char* AbO = AbE - nq * 4096;
#pragma unroll
        for (int c = 0; c < 4; ++c) {
            if (c == 0) WAIT_V(6); else if (c == 1) WAIT_V(4); else if (c == 2) WAIT_V(2); else WAIT_V(0);
            LBAR();
#pragma unroll
            for (int kk = 0; kk < 4; ++kk) {
                u32x4 bb = tr_frag(((kk & 1) ? AbO : AbE) + (8 * c + kk) * 2048);
                acc = mfma32(wf[4 * c + kk], bb, acc);
                if ((kk & 1) == 0) own[kk >> 1] = bb;
            }
#pragma unroll
            for (int i = 0; i < 2; ++i) {
                const u32x4 v = own[i];
                s1a = dot2bf(v[0], ones, s1a); s1b = dot2bf(v[1], ones, s1b);
                q1a = dot2bf(v[0], v[0], q1a); q1b = dot2bf(v[1], v[1], q1b);
                s1a = dot2bf(v[2], ones, s1a); s1b = dot2bf(v[3], ones, s1b);
                q1a = dot2bf(v[2], v[2], q1a); q1b = dot2bf(v[3], v[3], q1b);
            }
        }
        hres = tr_frag(Ab + (4 * ct + 2 * nq + kh) * 2048);
        W3LOAD(20, 28);
        TS(8);
        {
            LAS char* p = lds + G_RED + wave * 2048 + lane * 16;
            if (kh == 0) { *(LAS f32x4*)(p) = f32x4{acc[8], acc[9], acc[10], acc[11]}; *(LAS f32x4*)(p + 1024) = f32x4{acc[12], acc[13], acc[14], acc[15]}; }
            else { *(LAS f32x4*)(p) = f32x4{acc[0], acc[1], acc[2], acc[3]}; *(LAS f32x4*)(p + 1024) = f32x4{acc[4], acc[5], acc[6], acc[7]}; }
        }
        float s1 = add_xor32(s1a + s1b), q1 = add_xor32(q1a + q1b);
        LAS char* ps = lds + G_PS + (tq * 32 + r32) * 32;
        if (h32 == 0) *(LAS f32x2*)(ps + (nq * 2 + kh) * 8) = f32x2{s1, q1};
        LBAR();
        TS(9);
        float fin[8];
        {
            const LAS char* p = lds + G_RED + (wave ^ 4) * 2048 + lane * 16;
            f32x4 a0 = *(const LAS f32x4*)(p), a1 = *(const LAS f32x4*)(p + 1024);
#pragma unroll
            for (int e = 0; e < 4; ++e) {
                fin[e] = a0[e] + (kh ? acc[8 + e] : acc[e]);
                fin[4 + e] = a1[e] + (kh ? acc[12 + e] : acc[4 + e]);
            }
        }
        float m1, sd1, m2, sd2;
        f32x4 p0 = *(const LAS f32x4*)(ps), p1 = *(const LAS f32x4*)(ps + 16);
        const float mean = ((p0[0] + p0[2]) + (p1[0] + p1[2])) * (1.0f / 512);
        const float msq = ((p0[1] + p0[3]) + (p1[1] + p1[3])) * (1.0f / 512);
        const float var = fmaxf(msq - mean * mean, 0.f);
        ln_consts(mean, var, m1, sd1);
        const float r1 = __builtin_amdgcn_rcpf(sd1), nb1 = -m1 * r1;
        ln_consts((mean - m1) * r1, var * r1 * r1, m2, sd2);
        const float alpha = r1 * __builtin_amdgcn_rcpf(sd2);
        const float mu = m1 + m2 * sd1;
        const float ga = GC1 * alpha, gb = GC3 * alpha * alpha * alpha;
        u32x4 o;
#pragma unroll
        for (int e = 0; e < 4; ++e) {
            float d0_ = __builtin_fmaf(-mu, csv[2 * e], fin[2 * e]), d1_ = __builtin_fmaf(-mu, csv[2 * e + 1], fin[2 * e + 1]);
            o[e] = pk_bf16(gelu_s(d0_, alpha, ga, gb), gelu_s(d1_, alpha, ga, gb));
        }
        __builtin_amdgcn_raw_buffer_store_b128(o, rs, WS_HG + (((tt * 2 + tq) * 32 + 4 * ct + 2 * nq + kh) * 64 + lane) * 16, 0, 16);
#pragma unroll
        for (int e = 0; e < 4; ++e)
            x1res[e] = pk_bf16(__builtin_fmaf(bf_lo(hres[e]), r1, nb1), __builtin_fmaf(bf_hi(hres[e]), r1, nb1));
    }
    WAIT_V(0);
    LBAR();
    if (tid == 0) __hip_atomic_store((GAS u32*)(ws + WS_F2) + (tt * 8 + ct) * 32, ep, __ATOMIC_RELAXED, __HIP_MEMORY_SCOPE_AGENT);
    TS(10);

    {
        const LAS char* Wb = lds + A_W + kh * 8192 + (tr_off ^ (nq << 6));
#pragma unroll
        for (int i = 0; i < 16; ++i) wf[i] = tr_frag(Wb + ((i >> 2) * 8 + (i & 3)) * 2048);
    }
    if (wave < 7) CS3(20, 28);
    if (wave == 7) {
        const int tl = tt - 1 + (lane >> 3);
        const bool act = lane < 24 && tl >= 0 && tl < 32;
        const GAS u32* fp = (const GAS u32*)(ws + WS_F2) + ((act ? tl : tt) * 8 + (lane & 7)) * 32;
        _Pragma("unroll 1") for (int spin = 0; spin < SPIN_MAX; ++spin) {
            u32 f = __hip_atomic_load(fp, __ATOMIC_RELAXED, __HIP_MEMORY_SCOPE_AGENT);
            if (__all(f == ep)) break;
            __builtin_amdgcn_s_sleep(1);
        }
    }
    LBAR();
    TS(11);
#pragma unroll
    for (int c = 0; c < 4; ++c) {
        const int ks = 8 * c + wave;
        const int sh = c == 0 ? -1 : (c == 1 ? 1 : (c == 2 ? -2 : 2));
#pragma unroll
        for (int i = 0; i < 2; ++i) {
            const int t = tt * 64 + i * 32 + r32 + sh;
            const bool ok = (unsigned)t < (unsigned)L;
            const char* src = ok ? ws + WS_HG + ((((t >> 5) * 32 + ks) * 64) + h32 * 32 + (t & 31)) * 16 : (const char*)g_zero16;
            dma_g(src, lbase + G_ACT + (i * 32 + ks) * 1024);
        }
    }
    {
        f32x16 acc = {};
#pragma unroll
        for (int c = 0; c < 4; ++c) {
            if (c == 0) WAIT_V(6); else if (c == 1) WAIT_V(4); else if (c == 2) WAIT_V(2); else WAIT_V(0);
            LBAR();
#pragma unroll
            for (int kk = 0; kk < 4; ++kk) {
                const int ks = 8 * c + 4 * kh + kk;
                u32x4 bb = *(const LAS u32x4*)(lds + G_ACT + (tq * 32 + ks) * 1024 + lane * 16);
                acc = mfma32(wf[4 * c + kk], bb, acc);
            }
        }
        W3LOAD(28, 32);
        {
            LAS char* p = lds + G_RED + wave * 2048 + lane * 16;
            if (kh == 0) { *(LAS f32x4*)(p) = f32x4{acc[8], acc[9], acc[10], acc[11]}; *(LAS f32x4*)(p + 1024) = f32x4{acc[12], acc[13], acc[14], acc[15]}; }
            else { *(LAS f32x4*)(p) = f32x4{acc[0], acc[1], acc[2], acc[3]}; *(LAS f32x4*)(p + 1024) = f32x4{acc[4], acc[5], acc[6], acc[7]}; }
        }
        LBAR();
        TS(12);
        const LAS char* p = lds + G_RED + (wave ^ 4) * 2048 + lane * 16;
        f32x4 a0 = *(const LAS f32x4*)(p), a1 = *(const LAS f32x4*)(p + 1024);
        float fin[8];
#pragma unroll
        for (int e = 0; e < 4; ++e) {
            fin[e] = a0[e] + (kh ? acc[8 + e] : acc[e]);
            fin[4 + e] = a1[e] + (kh ? acc[12 + e] : acc[4 + e]);
        }
#pragma unroll
        for (int e = 0; e < 4; ++e)
            x1res[e] = pk_bf16v(unpk(x1res[e]) + unpk(pk_bf16(fin[2 * e], fin[2 * e + 1])));
        __builtin_amdgcn_raw_buffer_store_b128(x1res, rs, WS_X1 + (((tt * 2 + tq) * 32 + 4 * ct + 2 * nq + kh) * 64 + lane) * 16, 0, 16);
        {
            float sa = 0.f, qa = 0.f, sb = 0.f, qb = 0.f;
#pragma unroll
            for (int e = 0; e < 4; ++e) {
                const float lo = bf_lo(x1res[e]), hi = bf_hi(x1res[e]);
                sa += lo; sb += hi; qa = __builtin_fmaf(lo, lo, qa); qb = __builtin_fmaf(hi, hi, qb);
            }
            const float st = add_xor32(sa + sb), qt = add_xor32(qa + qb);
            if (h32 == 0)
                __builtin_amdgcn_raw_buffer_store_b64(u32x2{__float_as_uint(st), __float_as_uint(qt)}, rs,
                                                      WS_PST + ((tt * 2 + tq) * 32 + ct * 4 + nq * 2 + kh) * 256 + r32 * 8, 0, 16);
        }
        *(LAS u32x4*)(lds + G_SPARE + wave * 2048 + lane * 16) = x1res;
        *(LAS u32x4*)(lds + G_SPARE + wave * 2048 + 1024 + lane * 16) = hres;
    }
    WAIT_V(0);
    LBAR();
    if (tid == 0) __hip_atomic_store((GAS u32*)(ws + WS_F3) + (tt * 8 + ct) * 32, ep, __ATOMIC_RELAXED, __HIP_MEMORY_SCOPE_AGENT);
    TS(13);
    const int kq = 2 * tq + kh;
    const u32 w4base = WS_WT4 + ((ct * 2 + nq) * 112 + kq * 28) * 1024;
    const u32 w4st = wave < 2 ? 65536 + wave * 8192 : (wave < 4 ? 81920 + wave * 8192 : 98304 + wave * 8192);
    dma_ws4(srd_ws, lane * 16, w4base + 20 * 1024, lbase + w4st);
    dma_ws4(srd_ws, lane * 16, w4base + 24 * 1024, lbase + w4st + 4096);

    const u32 w4off = w4base + lane * 16;
    u32x4 w4f[28];
    const GAS u32* f4p = (const GAS u32*)(ws + WS_F4) + (tt * 8 + (lane & 7)) * 32;
#define ACT3_DMA() do { \
        dma_ws4(srd_ws, lane * 16, WS_X1 + tt * 65536 + wave * 4096, lbase + G_ACT + wave * 4096); \
        dma_ws4(srd_ws, lane * 16, WS_X1 + tt * 65536 + 32768 + wave * 4096, lbase + G_ACT + 32768 + wave * 4096); } while (0)
#define POLL(ptr) do { _Pragma("unroll 1") for (int spin_ = 0; spin_ < SPIN_MAX; ++spin_) { \
        u32 f_ = __hip_atomic_load((ptr), __ATOMIC_RELAXED, __HIP_MEMORY_SCOPE_AGENT); \
        if (__all(f_ == ep)) break; \
        __builtin_amdgcn_s_sleep(1); } } while (0)
    if (wave == 7) {
        POLL((const GAS u32*)(ws + WS_F3) + (tt * 8 + (lane & 7)) * 32);
        LBAR();
        ACT3_DMA();
        u32x2 pp[2][16];
#pragma unroll
        for (int i = 0; i < 2; ++i)
#pragma unroll
            for (int j = 0; j < 16; ++j)
                pp[i][j] = __builtin_amdgcn_raw_buffer_load_b64(rs, WS_PST + ((tt * 2 + i) * 32 + h32 * 16 + j) * 256 + r32 * 8, 0, 16);
        FENCE();
        WAIT_V(36);
        LBAR();
#pragma unroll
        for (int i = 0; i < 2; ++i) {
            float sa = 0.f, qa = 0.f, sb = 0.f, qb = 0.f;
#pragma unroll
            for (int j = 0; j < 16; j += 2) {
                sa += __uint_as_float(pp[i][j][0]); qa += __uint_as_float(pp[i][j][1]);
                sb += __uint_as_float(pp[i][j + 1][0]); qb += __uint_as_float(pp[i][j + 1][1]);
            }
            float mean = add_xor32(sa + sb) * (1.0f / 512), msq = add_xor32(qa + qb) * (1.0f / 512);
            float m, sd;
            ln_consts(mean, fmaxf(msq - mean * mean, 0.f), m, sd);
            if (h32 == 0) *(LAS f32x2*)(lds + G_PS + (i * 32 + r32) * 8) = f32x2{__builtin_amdgcn_rcpf(sd), m};
        }
        FENCE();
#pragma unroll
        for (int j = 0; j < 10; ++j) { w4f[j] = __builtin_amdgcn_raw_buffer_load_b128(rs, w4off + j * 1024, 0, 16); __builtin_amdgcn_s_sleep(2); }
        FENCE();
        LBAR();
#pragma unroll
        for (int j = 10; j < 20; ++j) { w4f[j] = __builtin_amdgcn_raw_buffer_load_b128(rs, w4off + j * 1024, 0, 16); __builtin_amdgcn_s_sleep(2); }
        FENCE();
#pragma unroll
        for (int j = 0; j < 8; ++j) w4f[20 + j] = *(const LAS u32x4*)(lds + w4st + j * 1024 + lane * 16);
        LBAR();
        POLL(f4p);
        LBAR();
    } else {
        CS3(28, 32);
        {
            const float csum = add_xor32(c3a + c3b);
            if (h32 == 0) *(LAS float*)(lds + G_CS + (wave * 32 + r32) * 4) = csum;
        }
        LBAR();
        TS(14);
        ACT3_DMA();
        const LAS char* Ab = lds + G_ACT + lane * 16;
        const u32 gdst = WS_G + ((tt * 2 * 112 + (ct * 7 + wave) * 2) * 64 + lane) * 16;
        f32x16 acc0 = {}, acc1 = {};
        u32x4 bq[4];
        WAIT_V(4);
        LBAR();
        TS(30);
#pragma unroll
        for (int i = 0; i < 4; ++i) bq[i] = *(const LAS u32x4*)(Ab + i * 1024);
#pragma unroll
        for (int ks = 0; ks < 32; ++ks) {
            acc0 = mfma32(w3f[ks], bq[ks & 3], acc0);
            if (ks + 4 < 32) bq[ks & 3] = *(const LAS u32x4*)(Ab + (ks + 4) * 1024);
            if (ks % 3 == 0) w4f[ks / 3] = __builtin_amdgcn_raw_buffer_load_b128(rs, w4off + (ks / 3) * 1024, 0, 16);
            SCHED0();
        }
        TS(31);
        WAIT_V(11);
        LBAR();
        TS(15);
#pragma unroll
        for (int i = 0; i < 4; ++i) bq[i] = *(const LAS u32x4*)(Ab + (32 + i) * 1024);
        float cs[16];
#pragma unroll
        for (int i = 0; i < 4; ++i) {
            f32x4 c4 = *(const LAS f32x4*)(lds + G_CS + (wave * 32 + 8 * i + 4 * h32) * 4);
            cs[4 * i] = c4[0]; cs[4 * i + 1] = c4[1]; cs[4 * i + 2] = c4[2]; cs[4 * i + 3] = c4[3];
        }
        {
            const f32x2 st = *(const LAS f32x2*)(lds + G_PS + r32 * 8);
            const float ga = GC1 * st.x, gb = GC3 * st.x * st.x * st.x;
            float g[16];
            SCHED0();
#pragma unroll
            for (int k4 = 0; k4 < 32; k4 += 4) {
#pragma unroll
                for (int ks = k4; ks < k4 + 4; ++ks) {
                    acc1 = mfma32(w3f[ks], bq[ks & 3], acc1);
                    if (ks + 4 < 32) bq[ks & 3] = *(const LAS u32x4*)(Ab + (32 + ks + 4) * 1024);
                    if (ks < 16) g[ks] = gelu_s(__builtin_fmaf(-st.y, cs[ks], acc0[ks]), st.x, ga, gb);
                    if (ks >= 20 && ks < 29) w4f[11 + ks - 20] = __builtin_amdgcn_raw_buffer_load_b128(rs, w4off + (11 + ks - 20) * 1024, 0, 16);
                    if (ks >= 24) w4f[20 + ks - 24] = *(const LAS u32x4*)(lds + w4st + (ks - 24) * 1024 + lane * 16);
                }
                if (k4 == 8 || k4 == 16) {
                    const int o = k4 - 8;
                    u32x4 v = {pk_bf16(g[o], g[o + 1]), pk_bf16(g[o + 2], g[o + 3]), pk_bf16(g[o + 4], g[o + 5]), pk_bf16(g[o + 6], g[o + 7])};
                    __builtin_amdgcn_raw_buffer_store_b128(v, rs, gdst + (o >> 3) * 1024, 0, 16);
                }
#pragma unroll
                for (int q = 0; q < 4; ++q) {
                    __builtin_amdgcn_sched_group_barrier(0x008, 1, 0);
                    __builtin_amdgcn_sched_group_barrier(0x100, 1, 0);
                    __builtin_amdgcn_sched_group_barrier(0x002, 10, 0);
                    __builtin_amdgcn_sched_group_barrier(0x010, 1, 0);
                }
                SCHED0();
            }
        }
        WAIT_V(9);
        LBAR();
        if (tid == 0) __hip_atomic_store((GAS u32*)(ws + WS_F4) + (tt * 8 + ct) * 32, ep, __ATOMIC_RELAXED, __HIP_MEMORY_SCOPE_AGENT);
        TS(16);
        {
            const f32x2 st = *(const LAS f32x2*)(lds + G_PS + (32 + r32) * 8);
            const float ga = GC1 * st.x, gb = GC3 * st.x * st.x * st.x;
#pragma unroll
            for (int sI = 0; sI < 2; ++sI) {
                u32x4 v;
#pragma unroll
                for (int e = 0; e < 4; ++e) {
                    const int j = 8 * sI + 2 * e;
                    v[e] = pk_bf16(gelu_s(__builtin_fmaf(-st.y, cs[j], acc1[j]), st.x, ga, gb),
                                   gelu_s(__builtin_fmaf(-st.y, cs[j + 1], acc1[j + 1]), st.x, ga, gb));
                }
                __builtin_amdgcn_raw_buffer_store_b128(v, rs, gdst + 112 * 1024 + sI * 1024, 0, 16);
            }
        }
        TS(17);
        LBAR();
    }
    TS(18);
    {
        const u32 gsrc = WS_G + (tt * 2 * 112 + wave * 14) * 1024;
        const u32 ldst = lbase + wave * 14336;
#define FILL_A(t) do { dma_ws4(srd_ws, lane * 16, gsrc + (t) * 114688, ldst); dma_ws4(srd_ws, lane * 16, gsrc + (t) * 114688 + 4096, ldst + 4096); } while (0)
#define FILL_B(t, dst) do { dma_ws4(srd_ws, lane * 16, gsrc + (t) * 114688 + 8192, (dst)); dma_ws2(srd_ws, lane * 16, gsrc + (t) * 114688 + 12288, (dst) + 4096); } while (0)
        FILL_A(0); FILL_B(0, ldst + 8192);
        x1res = *(const LAS u32x4*)(lds + G_SPARE + wave * 2048 + lane * 16);
        hres = *(const LAS u32x4*)(lds + G_SPARE + wave * 2048 + 1024 + lane * 16);
        WAIT_V(14);
        LBAR();
        if (tid == 0) __hip_atomic_store((GAS u32*)(ws + WS_F5) + (tt * 8 + ct) * 32, ep, __ATOMIC_RELAXED, __HIP_MEMORY_SCOPE_AGENT);
        const LAS char* Gb = lds + kq * 28672 + lane * 16;
        const LAS char* Gs = lds + G_SPARE + kq * 12288 + lane * 16;
        f32x16 acc0 = {}, acc1 = {};
        u32x4 bq[4];
#define KA(i) ((i) < 8 ? (i) : (i) + 6)
#define KB(i) ((i) < 6 ? (i) + 8 : (i) + 16)
#define KS(i) (i)
#define PART(ACC, BASE, KF, KW, N) do { \
        _Pragma("unroll") for (int i_ = 0; i_ < 4; ++i_) bq[i_] = *(const LAS u32x4*)(BASE + KF(i_) * 1024); \
        _Pragma("unroll") for (int i_ = 0; i_ < N; ++i_) { \
            ACC = mfma32(w4f[KW(i_)], bq[i_ & 3], ACC); \
            if (i_ + 4 < N) bq[i_ & 3] = *(const LAS u32x4*)(BASE + KF(i_ + 4) * 1024); \
            SCHED0(); } } while (0)
        WAIT_V(6);
        LBAR();
        TS(20);
        PART(acc0, Gb, KA, KA, 16);
        if (wave == 7) POLL(f4p + 8192);
        WAIT_V(0);
#pragma unroll
        for (int i = 0; i < 12; ++i) PIN4(w4f[KB(i)]);
        LBAR();
        TS(21);
        FILL_A(1); FILL_B(1, lbase + G_SPARE + wave * 6144);
        PART(acc0, Gb, KB, KB, 12);
        WAIT_V(6);
        LBAR();
        TS(22);
        PART(acc1, Gb, KA, KA, 16);
        WAIT_V(0);
        LBAR();
        TS(23);
        PART(acc1, Gs, KS, KB, 12);
        TS(24);
#pragma unroll
        for (int p = 0; p < 4; ++p) {
            if (p != kq) {
                const int slot = kq > p ? kq - 1 : kq;
                LAS char* dst = lds + ((nq * 4 + p) * 3 + slot) * 2048 + lane * 16;
                const int o = 8 * (p & 1);
                if (p < 2) { *(LAS f32x4*)(dst) = f32x4{acc0[o], acc0[o + 1], acc0[o + 2], acc0[o + 3]};
                             *(LAS f32x4*)(dst + 1024) = f32x4{acc0[o + 4], acc0[o + 5], acc0[o + 6], acc0[o + 7]}; }
                else { *(LAS f32x4*)(dst) = f32x4{acc1[o], acc1[o + 1], acc1[o + 2], acc1[o + 3]};
                       *(LAS f32x4*)(dst + 1024) = f32x4{acc1[o + 4], acc1[o + 5], acc1[o + 6], acc1[o + 7]}; }
            }
        }
        float fin[8];
#pragma unroll
        for (int e = 0; e < 8; ++e) {
            float lo = tq ? acc1[e] : acc0[e], hi = tq ? acc1[8 + e] : acc0[8 + e];
            fin[e] = kh ? hi : lo;
        }
        PIN4(hres); PIN4(x1res);
        LBAR();
#pragma unroll
        for (int sl = 0; sl < 3; ++sl) {
            const LAS char* src = lds + ((nq * 4 + kq) * 3 + sl) * 2048 + lane * 16;
            f32x4 a0 = *(const LAS f32x4*)(src), a1 = *(const LAS f32x4*)(src + 1024);
#pragma unroll
            for (int e = 0; e < 4; ++e) { fin[e] += a0[e]; fin[4 + e] += a1[e]; }
        }
        u32 o[4];
#pragma unroll
        for (int e = 0; e < 4; ++e) {
            f32x2 h2 = unpk(pk_bf16(fin[2 * e], fin[2 * e + 1]));
            f32x2 t1 = unpk(pk_bf16v(unpk(x1res[e]) + h2));
            o[e] = pk_bf16v(unpk(hres[e]) + t1);
        }
        int tz = threadIdx.x;
        asm volatile("" : "+v"(tz));
        u16* orow = out + (size_t)(tt * 64 + tq * 32 + (tz & 31)) * D + ct * 64 + nq * 32 + kh * 16 + 4 * ((tz >> 5) & 1);
        *(u32x2*)(orow) = u32x2{o[0], o[1]};
        *(u32x2*)(orow + 8) = u32x2{o[2], o[3]};
    }
    TS(25);
}

extern "C" void kernel_launch(void* const* d_in, const int* in_sizes, int n_in, void* d_out, int out_size,
                              void* d_ws, size_t ws_size, hipStream_t stream) {
    (void)hipFuncSetAttribute((const void*)k_mega, hipFuncAttributeMaxDynamicSharedMemorySize, LDS_BYTES);
    k_mega<<<256, 512, LDS_BYTES, stream>>>((const u16*)d_in[0], (const u16*)d_in[1], (const u16*)d_in[2],
                                            (const u16*)d_in[3], (const u16*)d_in[4], (const u16*)d_in[5],
                                            (const u16*)d_in[6], (const u16*)d_in[7], (const u16*)d_in[8],
                                            (const u16*)d_in[10], (const u16*)d_in[11], (const u16*)d_in[12],
                                            (const u16*)d_in[13], (u16*)d_out, (char*)d_ws);
}
```

```cpp
#include <hip/hip_runtime.h>
#include <hip/hip_bf16.h>
#include <stdint.h>

#define DEV __device__ __forceinline__
#define LAS __attribute__((address_space(3)))
#define GAS __attribute__((address_space(1)))
typedef unsigned int u32;
typedef unsigned short u16;
typedef unsigned long long u64;
typedef float f32x2 __attribute__((ext_vector_type(2)));
typedef float f32x4 __attribute__((ext_vector_type(4)));
typedef float f32x16 __attribute__((ext_vector_type(16)));
typedef u32 u32x2 __attribute__((ext_vector_type(2)));
typedef u32 u32x4 __attribute__((ext_vector_type(4)));
typedef short s16x4 __attribute__((ext_vector_type(4)));
typedef __bf16 bf16x2 __attribute__((ext_vector_type(2)));
typedef __bf16 bf16x8 __attribute__((ext_vector_type(8)));
typedef _Float16 f16x2 __attribute__((ext_vector_type(2)));
typedef _Float16 f16x8 __attribute__((ext_vector_type(8)));

#define TS(i)

constexpr int L = 2048, D = 512, NS = 128, F = 1792;

constexpr u32 WS_CTL = 0;
constexpr u32 WS_F1 = 8192;
constexpr u32 WS_F0 = 16384;
constexpr u32 WS_F2 = 32768;
constexpr u32 WS_F3 = 65536;
constexpr u32 WS_F4 = 98304;
constexpr u32 WS_F5 = 131072;
constexpr u32 WS_WT3 = 163840;
constexpr u32 WS_WT4 = WS_WT3 + 512 * 1792 * 2;
constexpr u32 WS_HC = WS_WT4 + 512 * 1792 * 2;
constexpr u32 WS_HG = WS_HC + 2048 * 512 * 2;
constexpr u32 WS_X1 = WS_HG + 2048 * 512 * 2;
constexpr u32 WS_G = WS_X1 + 2048 * 512 * 2;
constexpr u32 WS_XU = WS_G + 2048 * 1792 * 2;
constexpr u32 WS_PST = WS_XU + 16384;
constexpr u32 WS_END = WS_PST + 64 * 8192;

__device__ const u32x4 g_zero16[4] = {};

constexpr int c_round_bf16(int Lg) {
    if (Lg < 256) return Lg;
    int e = 0;
    while ((Lg >> (e + 1)) != 0) ++e;
    int sh = e - 7, q = Lg >> sh, rem = Lg & ((1 << sh) - 1), half = 1 << (sh - 1);
    if (rem > half || (rem == half && (q & 1))) ++q;
    return q << sh;
}
constexpr int c_ci(int Lr) {
    if (Lr < 256) return Lr;
    int e = 0;
    while ((Lr >> (e + 1)) != 0) ++e;
    return 128 * (e - 6) + ((Lr >> (e - 7)) - 128);
}
constexpr int KC_ZERO = 644, KC_DUMMY = 648, KC_LEN = 656;
struct Tabs {
    u16 ci[3 * 64 * 4];
    u16 ex[256 * 12];
    constexpr Tabs() : ci(), ex() {
        for (int mt = 0; mt < 3; ++mt)
            for (int l = 0; l < 64; ++l)
                for (int e = 0; e < 4; ++e) {
                    int row = mt * 16 + (l >> 4) * 4 + e, j = l & 15, lrow = 0;
                    if (row < 16) lrow = (row & 7) + 128 * (row >> 3);
                    else if (row < 24) lrow = 256 + 2 * (row & 3) + 128 * ((row >> 2) & 1);
                    else if (row < 32) lrow = 512 + 4 * (row & 1) + 128 * ((row >> 1) & 3);
                    else if (row < 40) lrow = 1024 + 128 * (row & 7);
                    else lrow = 2048;
                    bool valid = row < 40 || (row == 40 && j == 0);
                    ci[(mt * 64 + l) * 4 + e] = (u16)(2 * (valid ? c_ci(lrow + 8 * j) : KC_DUMMY));
                }
        for (int c = 0; c < 256; ++c)
            for (int i = 0; i < 12; ++i) {
                int lag = 2047 - (8 * c + i);
                ex[c * 12 + i] = (u16)(2 * (lag < 0 ? KC_ZERO : c_ci(c_round_bf16(lag))));
            }
    }
};
__device__ const Tabs g_tabs = Tabs();

DEV float bf_lo(u32 v) { return __uint_as_float(v << 16); }
DEV float bf_hi(u32 v) { return __uint_as_float(v & 0xffff0000u); }
DEV float bf1(u16 v) { return __uint_as_float(((u32)v) << 16); }
DEV u32 pk_bf16(float a, float b) {
    f32x2 f = {a, b};
    bf16x2 r = __builtin_convertvector(f, bf16x2);
    return __builtin_bit_cast(u32, r);
}
DEV u32 pk_bf16v(f32x2 f) {
    bf16x2 r = __builtin_convertvector(f, bf16x2);
    return __builtin_bit_cast(u32, r);
}
DEV f32x2 unpk(u32 v) { return f32x2{bf_lo(v), bf_hi(v)}; }
DEV float rbf(float a) { return bf_lo(pk_bf16(a, 0.f)); }
DEV u32 pk_f16(float a, float b) {
    f32x2 f = {a, b};
    f16x2 r = __builtin_convertvector(f, f16x2);
    return __builtin_bit_cast(u32, r);
}
DEV float dot2bf(u32 a, u32 b, float c) {
    return __builtin_amdgcn_fdot2_f32_bf16(__builtin_bit_cast(bf16x2, a), __builtin_bit_cast(bf16x2, b), c, false);
}
DEV f32x16 mfma32(u32x4 a, u32x4 b, f32x16 c) {
    return __builtin_amdgcn_mfma_f32_32x32x16_bf16(__builtin_bit_cast(bf16x8, a), __builtin_bit_cast(bf16x8, b), c, 0, 0, 0);
}
DEV f32x4 mfma16h(u32x4 a, u32x4 b, f32x4 c) {
    return __builtin_amdgcn_mfma_f32_16x16x32_f16(__builtin_bit_cast(f16x8, a), __builtin_bit_cast(f16x8, b), c, 0, 0, 0);
}
DEV f32x2 gelu2(f32x2 x) {
    const float c1 = -2.0f * 0.7978845608028654f * 1.4426950408889634f;
    const float c3 = c1 * 0.044715f;
    f32x2 t = x * (x * x * c3 + c1);
    f32x2 e = {__builtin_amdgcn_exp2f(t.x), __builtin_amdgcn_exp2f(t.y)};
    e = e + 1.0f;
    f32x2 r = {__builtin_amdgcn_rcpf(e.x), __builtin_amdgcn_rcpf(e.y)};
    return x * r;
}
template <int CTRL, int RM>
DEV float dpp_f(float v) {
    return __int_as_float(__builtin_amdgcn_update_dpp(0, __float_as_int(v), CTRL, RM, 0xf, true));
}
DEV float wave_sum(float v) {
    v += dpp_f<0x111, 0xf>(v); v += dpp_f<0x112, 0xf>(v); v += dpp_f<0x114, 0xf>(v); v += dpp_f<0x118, 0xf>(v);
    v += dpp_f<0x142, 0xa>(v); v += dpp_f<0x143, 0xc>(v);
    return __int_as_float(__builtin_amdgcn_readlane(__float_as_int(v), 63));
}
DEV float wave_max(float v) {
    v = fmaxf(v, dpp_f<0x111, 0xf>(v)); v = fmaxf(v, dpp_f<0x112, 0xf>(v)); v = fmaxf(v, dpp_f<0x114, 0xf>(v));
    v = fmaxf(v, dpp_f<0x118, 0xf>(v)); v = fmaxf(v, dpp_f<0x142, 0xa>(v)); v = fmaxf(v, dpp_f<0x143, 0xc>(v));
    return __int_as_float(__builtin_amdgcn_readlane(__float_as_int(v), 63));
}
DEV float add_xor32(float v) {
    u32 u = __float_as_uint(v);
    auto sw = __builtin_amdgcn_permlane32_swap(u, u, false, false);
    u32 a = sw[0], b = sw[1];
    return __uint_as_float(a) + __uint_as_float(b);
}
#define WAIT_V(n) asm volatile("s_waitcnt vmcnt(" #n ")" ::: "memory")
#define FENCE() asm volatile("" ::: "memory")
#define LBAR() do { asm volatile("s_waitcnt lgkmcnt(0)" ::: "memory"); __builtin_amdgcn_s_barrier(); asm volatile("" ::: "memory"); } while (0)
#define PIN4(a) asm volatile("" : "+v"(a))

DEV void ln_consts(float mean, float var, float& m_bf, float& sd_bf) {
    m_bf = rbf(mean);
    float v = rbf(var);
    v = rbf(v + 1e-5f);
    sd_bf = rbf(__builtin_amdgcn_sqrtf(v));
}
DEV void dma_ws(u32x4 srd, u32 voff, u32 soff, u32 ldst) {
    asm volatile("s_mov_b32 m0, %0\n\ts_nop 0\n\tbuffer_load_dwordx4 %1, %2, %3 offen sc1 lds"
                 :: "s"(ldst), "v"(voff), "s"(srd), "s"(soff) : "memory");
}
DEV void dma_ws2(u32x4 srd, u32 voff, u32 soff, u32 ldst) {
    asm volatile("s_mov_b32 m0, %0\n\ts_nop 0\n\tbuffer_load_dwordx4 %1, %2, %3 offen sc1 lds\n\t"
                 "buffer_load_dwordx4 %1, %2, %3 offen offset:1024 sc1 lds"
                 :: "s"(ldst), "v"(voff), "s"(srd), "s"(soff) : "memory");
}
DEV void dma_ws4(u32x4 srd, u32 voff, u32 soff, u32 ldst) {
    asm volatile("s_mov_b32 m0, %0\n\ts_nop 0\n\tbuffer_load_dwordx4 %1, %2, %3 offen sc1 lds\n\t"
                 "buffer_load_dwordx4 %1, %2, %3 offen offset:1024 sc1 lds\n\t"
                 "buffer_load_dwordx4 %1, %2, %3 offen offset:2048 sc1 lds\n\t"
                 "buffer_load_dwordx4 %1, %2, %3 offen offset:3072 sc1 lds"
                 :: "s"(ldst), "v"(voff), "s"(srd), "s"(soff) : "memory");
}
DEV void dma_in(u32x4 srd, u32 voff, u32 soff, u32 ldst) {
    asm volatile("s_mov_b32 m0, %0\n\ts_nop 0\n\tbuffer_load_dwordx4 %1, %2, %3 offen lds"
                 :: "s"(ldst), "v"(voff), "s"(srd), "s"(soff) : "memory");
}
DEV void dma_g(const void* g, u32 ldst) {
    asm volatile("s_mov_b32 m0, %0\n\ts_nop 0\n\tglobal_load_lds_dwordx4 %1, off sc1"
                 :: "s"(ldst), "v"(g) : "memory");
}
DEV u32x4 make_srd(const void* p, u32 bytes) {
    u64 a = (u64)p;
    return u32x4{(u32)a, (u32)(a >> 32) & 0xffffu, bytes, 0x00020000u};
}
#define SCHED0() __builtin_amdgcn_sched_barrier(0)
DEV u32x4 tr_frag(const LAS char* p) {
    s16x4 a = __builtin_amdgcn_ds_read_tr16_b64_v4i16((LAS s16x4*)p);
    s16x4 b = __builtin_amdgcn_ds_read_tr16_b64_v4i16((LAS s16x4*)(p + 1024));
    u32x2 x = __builtin_bit_cast(u32x2, a), y = __builtin_bit_cast(u32x2, b);
    return u32x4{x[0], x[1], y[0], y[1]};
}

struct cplx { float x, y; };
DEV cplx cmul(cplx a, cplx b) {
    cplx r;
    asm("v_mul_f32 %0, %3, %5\n\tv_mul_f32 %1, %3, %4\n\tv_fma_f32 %0, %2, %4, -%0\n\tv_fma_f32 %1, %2, %5, %1"
        : "=&v"(r.x), "=&v"(r.y) : "v"(a.x), "v"(a.y), "v"(b.x), "v"(b.y));
    return r;
}
DEV cplx csq(cplx a) {
    cplx r;
    asm("v_mul_f32 %0, %3, %3\n\tv_mul_f32 %1, %2, %3\n\tv_fma_f32 %0, %2, %2, -%0\n\tv_add_f32 %1, %1, %1"
        : "=&v"(r.x), "=&v"(r.y) : "v"(a.x), "v"(a.y));
    return r;
}
DEV cplx cexp_pow2(float tr, float wh, float wl, float p2) {
    float mag = __builtin_amdgcn_exp2f(tr * p2);
    float ph = __builtin_amdgcn_fractf(wh * p2) + wl * p2;
    return cplx{mag * __builtin_amdgcn_cosf(ph), mag * __builtin_amdgcn_sinf(ph)};
}
template <int CTRL, int RM>
DEV u32 dpp_u(u32 v) { return (u32)__builtin_amdgcn_update_dpp(0, (int)v, CTRL, RM, 0xf, true); }
DEV u32 wave_max_u(u32 v) {
    v = max(v, dpp_u<0x111, 0xf>(v)); v = max(v, dpp_u<0x112, 0xf>(v)); v = max(v, dpp_u<0x114, 0xf>(v));
    v = max(v, dpp_u<0x118, 0xf>(v)); v = max(v, dpp_u<0x142, 0xa>(v)); v = max(v, dpp_u<0x143, 0xc>(v));
    return (u32)__builtin_amdgcn_readlane((int)v, 63);
}
DEV float gelu_s(float d, float r, float ga, float gb) {
    float t = d * __builtin_fmaf(d * d, gb, ga);
    float e = __builtin_amdgcn_exp2f(t);
    return (d * r) * __builtin_amdgcn_rcpf(1.0f + e);
}
constexpr float GC1 = -2.0f * 0.7978845608028654f * 1.4426950408889634f, GC3 = GC1 * 0.044715f;

constexpr int TRS = 528;
constexpr int RCOPY = 4160;
constexpr int A_R = 0;
constexpr int A_U = 33280, UPAD = 240, ULEN = 2304;
constexpr int A_KC = A_U + 2 * ULEN * 2;
constexpr int A_X = 45312, A_XCH = 41 * TRS;
constexpr int A_Y = A_X + 2 * A_XCH, A_YCH = 16 * TRS;
constexpr int A_SCR = 107520;
constexpr int A_YP = 45312;
constexpr int A_W = 98304;
constexpr int A_XI = 123904, XI_RS = 1088;
constexpr int LDS_BYTES = 163840;
constexpr int G_ACT = 0, G_RED = 65536, G_PS = 81920, G_CS = 86016, G_SPARE = 114688;
constexpr int SPIN_MAX = 1 << 18;

__global__ __launch_bounds__(512, 2) void k_mega(const u16* __restrict__ x, const u16* __restrict__ A_re,
                                                 const u16* __restrict__ A_im, const u16* __restrict__ B_re,
                                                 const u16* __restrict__ B_im, const u16* __restrict__ C_re,
                                                 const u16* __restrict__ C_im, const u16* __restrict__ log_dt,
                                                 const u16* __restrict__ Dskip, const u16* __restrict__ w1,
                                                 const u16* __restrict__ w2, const u16* __restrict__ w3,
                                                 const u16* __restrict__ w4, u16* __restrict__ out, char* ws) {
    extern __shared__ __attribute__((aligned(16))) char lds_raw[];
    LAS char* lds = (LAS char*)lds_raw;
    const int tid = threadIdx.x, lane = tid & 63, wave = __builtin_amdgcn_readfirstlane(tid >> 6);
    const int b = blockIdx.x, bq = b >> 3;
    const int cb = (b & 7) * 32 + bq, d0 = cb * 2;
    const int tt = (b & 7) * 4 + (bq >> 3), ct = bq & 7;
    const auto rs = __builtin_amdgcn_make_buffer_rsrc(ws, 0, WS_END, 0x00020000);
    const u32x4 srd_ws = make_srd(ws, WS_END), srd_w1 = make_srd(w1, D * D * 2), srd_w2 = make_srd(w2, D * D * 2);
    const u32 lbase = (u32)(size_t)lds;
    asm volatile("" :: "s"(x), "s"(A_re), "s"(A_im), "s"(B_re), "s"(B_im), "s"(C_re), "s"(C_im), "s"(log_dt), "s"(Dskip),
                 "s"(w1), "s"(w2), "s"(w3), "s"(w4), "s"(out), "s"(ws));
    TS(0);

    const u32 ep = *(const GAS u32*)(ws + WS_CTL) + 1u;
    u32x4 xr = *(const u32x4*)(x + (size_t)(b * 8 + wave) * D + lane * 8);
    const int kch = wave >> 2, knh = wave & 1, kpart = (wave >> 1) & 1;
    const int pidx = (d0 + kch) * NS + knh * 64 + lane;
    u16 p_are = A_re[pidx], p_aim = A_im[pidx], p_bre = B_re[pidx], p_bim = B_im[pidx], p_cre = C_re[pidx], p_cim = C_im[pidx];
    u16 o_bre = B_re[pidx ^ 64], o_bim = B_im[pidx ^ 64], o_cre = C_re[pidx ^ 64], o_cim = C_im[pidx ^ 64];
    u32 p_ldt = *(const u32*)(log_dt + d0);
    u32 p_dsk = *(const u32*)(Dskip + d0);
    u32x4 traw[2];
    u32 tdst = 0;
    {
        int pr = b * 7 + (wave < 7 ? wave : 0);
        const u16* src; int N;
        if (pr < 896) { int ntp = pr >> 5, ks = pr & 31; src = w3 + (size_t)(16 * ks) * F + ntp * 64; N = F;
                        tdst = WS_WT3 + ((ntp * 2) * 32 + ks) * 1024; }
        else { int p2 = pr - 896; int ntp = p2 / 112, ks = p2 - ntp * 112; src = w4 + (size_t)(16 * ks) * D + ntp * 64; N = D;
               tdst = WS_WT4 + ((ntp * 2) * 112 + ks) * 1024; }
#pragma unroll
        for (int i = 0; i < 2; ++i) traw[i] = *(const u32x4*)(src + (size_t)(8 * i + (lane >> 3)) * N + (lane & 7) * 8);
    }
    u32x2 tci = *(const u32x2*)(g_tabs.ci + ((wave & 3) < 3 ? (wave & 3) : 0) * 256 + lane * 4);
    u32x4 tex0 = *(const u32x4*)(g_tabs.ex + (tid & 255) * 12);
    u32x2 tex1 = *(const u32x2*)(g_tabs.ex + (tid & 255) * 12 + 8);

    FENCE();
    __builtin_amdgcn_s_barrier();
    u32 xv[4];
    const u16* xcol = x + (size_t)(tid * 4) * D + d0;
#define GATHER(k) do { FENCE(); xv[k] = *(const u32*)(xcol + (k) * D); FENCE(); } while (0)
    GATHER(0);
    const int trq = (lane & 15) >> 2, trp = lane & 3, trh = lane >> 5, trg = (lane >> 4) & 1;
    const int tr_off = (4 * trh + trq) * 128 + (((2 * trg + (trp >> 1)) ^ ((trq >> 1) << 2)) << 4) + 8 * (trp & 1);
    const int dma_sw = ((lane & 7) ^ (((lane >> 4) & 1) << 2)) * 8;
    {
        f32x2 f[4];
#pragma unroll
        for (int i = 0; i < 4; ++i) f[i] = unpk(xr[i]);
        f32x2 s2 = (f[0] + f[1]) + (f[2] + f[3]);
        float mean = wave_sum(s2.x + s2.y) * (1.0f / D);
        f32x2 q2 = {0.f, 0.f};
#pragma unroll
        for (int i = 0; i < 4; ++i) { f32x2 dd = f[i] - mean; q2 += dd * dd; }
        float var = wave_sum(q2.x + q2.y) * (1.0f / D);
        float m, sd;
        ln_consts(mean, var, m, sd);
        const u32 val = (__float_as_uint(m) >> 16) | (__float_as_uint(sd) & 0xffff0000u);
        if (lane == 0)
            __hip_atomic_store((GAS u64*)(ws + WS_XU) + (b * 8 + wave), ((u64)ep << 32) | val, __ATOMIC_RELAXED, __HIP_MEMORY_SCOPE_AGENT);
        GATHER(1);
        LAS char* scr = lds + A_SCR + wave * 2048;
#pragma unroll
        for (int i = 0; i < 2; ++i)
            *(LAS u32x4*)(scr + i * 1024 + (lane >> 3) * 128 + (((lane & 7) ^ (((lane >> 4) & 1) << 2)) << 4)) = traw[i];
        u32x4 f0 = tr_frag(scr + tr_off), f1 = tr_frag(scr + (tr_off ^ 64));
        if (wave < 7) {
            const u32 nstride = tdst < WS_WT4 ? 32 * 1024 : 112 * 1024;
            __builtin_amdgcn_raw_buffer_store_b128(f0, rs, tdst + lane * 16, 0, 16);
            __builtin_amdgcn_raw_buffer_store_b128(f1, rs, tdst + nstride + lane * 16, 0, 16);
        }
        FENCE();
    }
    TS(1);
    float inv_scale[2];
    {
        float dts[2] = {rbf(__expf(bf_lo(p_ldt))), rbf(__expf(bf_hi(p_ldt)))};
        float dt = kch ? dts[1] : dts[0];
        float zr = dt * bf1(p_are), zi = dt * bf1(p_aim);
        float tr = zr * 1.4426950408889634f;
        const float inv2pi_h = 0.15915494309189535f;
        const float inv2pi_l = (float)(0.15915494309189533576888 - (double)inv2pi_h);
        float wh = zi * inv2pi_h;
        float wl = __builtin_fmaf(zi, inv2pi_h, -wh) + zi * inv2pi_l;
        cplx CB = cmul(cplx{bf1(p_cre), bf1(p_cim)}, cplx{dt * bf1(p_bre), dt * bf1(p_bim)});
        cplx CO = cmul(cplx{bf1(o_cre), bf1(o_cim)}, cplx{dt * bf1(o_bre), dt * bf1(o_bim)});
        u32 mx = wave_max_u(max(max(__float_as_uint(CB.x) & 0x7fffffffu, __float_as_uint(CB.y) & 0x7fffffffu),
                                max(__float_as_uint(CO.x) & 0x7fffffffu, __float_as_uint(CO.y) & 0x7fffffffu)));
        int e = (int)(mx >> 23);
        e = e < 8 ? 8 : (e > 246 ? 246 : e);
        float sc = __uint_as_float((u32)(254 + 6 - e) << 23);
        CB.x *= sc; CB.y *= sc;
        if (lane == 0 && knh == 0 && kpart == 0) {
            *(LAS float*)(lds + A_KC + (kch * KC_LEN + 652) * 2) = __uint_as_float((u32)(e - 6) << 23);
            *(LAS u32*)(lds + A_KC + (kch * KC_LEN + KC_ZERO) * 2) = 0u;
        }
        GATHER(2);
        LAS char* Xb = lds + A_X + kch * A_XCH + (knh * 64 + lane) * 4;
        LAS char* Yb = lds + A_Y + kch * A_YCH + (knh * 64 + lane) * 4;
#define PUTX(row, v) *(LAS u32*)(Xb + (row) * TRS) = pk_f16((v).x, (v).y)
#define PUTY(row, v) *(LAS u32*)(Yb + (row) * TRS) = pk_f16((v).x, (v).y)
        if (kpart == 0) {
            cplx P1 = cexp_pow2(tr, wh, wl, 1.f), Q8 = cexp_pow2(tr, -wh, -wl, 8.f), P128 = cexp_pow2(tr, wh, wl, 128.f);
            cplx P2 = csq(P1), Q16 = csq(Q8), P4 = csq(P2), Q32 = csq(Q16), Q64 = csq(Q32);
            cplx Y[8];
            Y[0] = cplx{1.f, 0.f}; Y[1] = Q8; Y[2] = Q16; Y[3] = cmul(Q8, Q16); Y[4] = Q32;
            Y[5] = cmul(Q32, Q8); Y[6] = cmul(Q32, Q16); Y[7] = cmul(Q32, Y[3]);
#pragma unroll
            for (int j = 0; j < 8; ++j) PUTY(j, Y[j]);
            PUTY(8, Q64);
#pragma unroll
            for (int j = 1; j < 8; ++j) { cplx v = cmul(Q64, Y[j]); PUTY(8 + j, v); }
            cplx Xa[8];
            Xa[0] = CB; Xa[1] = cmul(CB, P1); Xa[2] = cmul(CB, P2); Xa[3] = cmul(Xa[2], P1);
#pragma unroll
            for (int j = 0; j < 4; ++j) Xa[4 + j] = cmul(Xa[j], P4);
#pragma unroll
            for (int j = 0; j < 8; ++j) { PUTX(j, Xa[j]); cplx v = cmul(Xa[j], P128); PUTX(8 + j, v); }
        } else {
            cplx P2 = cexp_pow2(tr, wh, wl, 2.f), P128 = cexp_pow2(tr, wh, wl, 128.f), P1024 = cexp_pow2(tr, wh, wl, 1024.f);
            cplx P4 = csq(P2), P256 = csq(P128), P2048 = csq(P1024), P512 = csq(P256);
            cplx X0 = CB, X2 = cmul(CB, P2), X4 = cmul(CB, P4), X6 = cmul(X2, P4);
            cplx X128 = cmul(CB, P128), X132 = cmul(X4, P128);
            cplx c0[4] = {cmul(X0, P256), cmul(X2, P256), cmul(X4, P256), cmul(X6, P256)};
            cplx c1[4];
#pragma unroll
            for (int j = 0; j < 4; ++j) c1[j] = cmul(c0[j], P128);
#pragma unroll
            for (int j = 0; j < 4; ++j) { PUTX(16 + j, c0[j]); PUTX(20 + j, c1[j]); }
            cplx dd[8] = {cmul(X0, P512), cmul(X4, P512), cmul(X128, P512), cmul(X132, P512),
                          cmul(c0[0], P512), cmul(c0[2], P512), cmul(c1[0], P512), cmul(c1[2], P512)};
#pragma unroll
            for (int j = 0; j < 8; ++j) PUTX(24 + j, dd[j]);
            cplx ee[8] = {X0, X128, c0[0], c1[0], dd[0], dd[2], dd[4], dd[6]};
#pragma unroll
            for (int j = 0; j < 8; ++j) { cplx v = cmul(ee[j], P1024); PUTX(32 + j, v); }
            cplx v = cmul(CB, P2048);
            PUTX(40, v);
        }
    }

    WAIT_V(1);
    LBAR();
    GATHER(3);
    if (tid < 8) __hip_atomic_store((GAS u32*)(ws + WS_F0) + tid * 256 + b, ep, __ATOMIC_RELAXED, __HIP_MEMORY_SCOPE_AGENT);
    TS(2);
    if ((wave & 3) < 3) {
        const int mt = wave & 3, r = lane & 15, g = lane >> 4;
        const LAS char* Xb = lds + A_X + kch * A_XCH + (mt * 16 + r) * TRS + g * 16;
        const LAS char* Yb = lds + A_Y + kch * A_YCH + r * TRS + g * 16;
        f32x4 acc0 = {0, 0, 0, 0}, acc1 = {0, 0, 0, 0};
#pragma unroll
        for (int ks = 0; ks < 8; ks += 2) {
            acc0 = mfma16h(*(const LAS u32x4*)(Xb + ks * 64), *(const LAS u32x4*)(Yb + ks * 64), acc0);
            acc1 = mfma16h(*(const LAS u32x4*)(Xb + ks * 64 + 64), *(const LAS u32x4*)(Yb + ks * 64 + 64), acc1);
        }
        acc0 += acc1;
        u32 k01 = pk_f16(acc0[0], acc0[1]), k23 = pk_f16(acc0[2], acc0[3]);
        LAS char* Kc = lds + A_KC + kch * KC_LEN * 2;
        *(LAS u16*)(Kc + (tci[0] & 0xffff)) = (u16)k01;
        *(LAS u16*)(Kc + (tci[0] >> 16)) = (u16)(k01 >> 16);
        *(LAS u16*)(Kc + (tci[1] & 0xffff)) = (u16)k23;
        *(LAS u16*)(Kc + (tci[1] >> 16)) = (u16)(k23 >> 16);
    }
    u32x4 g0 = __builtin_amdgcn_raw_buffer_load_b128(rs, WS_XU + tid * 32, 0, 16);
    u32x4 g1 = __builtin_amdgcn_raw_buffer_load_b128(rs, WS_XU + tid * 32 + 16, 0, 16);
    FENCE();
    LBAR();
    TS(3);
    {
        const int ch = tid >> 8, c = tid & 255;
        const LAS char* Kc = lds + A_KC + ch * KC_LEN * 2;
        u32 pe[6];
        u32 tw[6] = {tex0[0], tex0[1], tex0[2], tex0[3], tex1[0], tex1[1]};
#pragma unroll
        for (int i = 0; i < 6; ++i) {
            u32 lo = *(const LAS u16*)(Kc + (tw[i] & 0xffff));
            u32 hi = *(const LAS u16*)(Kc + (tw[i] >> 16));
            pe[i] = lo | (hi << 16);
        }
        u32 po[5];
#pragma unroll
        for (int i = 0; i < 5; ++i) po[i] = __builtin_amdgcn_alignbit(pe[i + 1], pe[i], 16);
        inv_scale[0] = *(const LAS float*)(lds + A_KC + 652 * 2);
        inv_scale[1] = *(const LAS float*)(lds + A_KC + (KC_LEN + 652) * 2);
        LAS char* Rb = lds + A_R + ch * 4 * RCOPY + c * 16;
        *(LAS u32x4*)(Rb + 0 * RCOPY) = u32x4{pe[0], pe[1], pe[2], pe[3]};
        *(LAS u32x4*)(Rb + 1 * RCOPY) = u32x4{po[0], po[1], po[2], po[3]};
        *(LAS u32x4*)(Rb + 2 * RCOPY) = u32x4{pe[1], pe[2], pe[3], pe[4]};
        *(LAS u32x4*)(Rb + 3 * RCOPY) = u32x4{po[1], po[2], po[3], po[4]};
        if (tid < 16) *(LAS u32x4*)(lds + A_R + (tid >> 1) * RCOPY + 4096 + (tid & 1) * 16) = u32x4{0, 0, 0, 0};
        if (tid >= 64 && tid < 124) {
            *(LAS u32x2*)(lds + A_U + (tid - 64) * 8) = u32x2{0, 0};
            *(LAS u32x2*)(lds + A_U + ULEN * 2 + (tid - 64) * 8) = u32x2{0, 0};
        }
    }

    _Pragma("unroll 1") for (int spin = 0; spin < SPIN_MAX; ++spin) {
        const bool ok = g0[1] == ep && g0[3] == ep && g1[1] == ep && g1[3] == ep;
        if (__all(ok)) break;
        __builtin_amdgcn_s_sleep(2);
        FENCE();
        g0 = __builtin_amdgcn_raw_buffer_load_b128(rs, WS_XU + tid * 32, 0, 16);
        g1 = __builtin_amdgcn_raw_buffer_load_b128(rs, WS_XU + tid * 32 + 16, 0, 16);
    }
    u32 udp[4];
    {
        const u32 st[4] = {g0[0], g0[2], g1[0], g1[2]};
        const f32x2 dsk = unpk(p_dsk);
        f32x2 u[4];
#pragma unroll
        for (int k = 0; k < 4; ++k) {
            const float m = bf_lo(st[k]), rsd = __builtin_amdgcn_rcpf(bf_hi(st[k]));
            const f32x2 dlt = unpk(pk_bf16v(unpk(xv[k]) - m));
            u[k] = unpk(pk_bf16v(dlt * rsd));
            udp[k] = pk_bf16v(u[k] * dsk);
        }
        *(LAS u32x2*)(lds + A_U + (UPAD + tid * 4) * 2) = u32x2{pk_f16(u[0].x, u[1].x), pk_f16(u[2].x, u[3].x)};
        *(LAS u32x2*)(lds + A_U + ULEN * 2 + (UPAD + tid * 4) * 2) = u32x2{pk_f16(u[0].y, u[1].y), pk_f16(u[2].y, u[3].y)};
    }
    if (wave == 7) {
        _Pragma("unroll 1") for (int spin = 0; spin < SPIN_MAX; ++spin) {
            FENCE();
            u32x4 f = __builtin_amdgcn_raw_buffer_load_b128(rs, WS_F0 + (b & 7) * 1024 + lane * 16, 0, 16);
            if (__all(f[0] == ep && f[1] == ep && f[2] == ep && f[3] == ep)) break;
            __builtin_amdgcn_s_sleep(2);
        }
    }
    const u32 w3off = wave < 7 ? WS_WT3 + (ct * 7 + wave) * 32768 + lane * 16 : 0xF0000000u;
    u32x4 w3f[32];
    float c3a = 0.f, c3b = 0.f;
#define CS3(lo, hi) do { _Pragma("unroll") for (int i_ = (lo); i_ < (hi); ++i_) { \
        c3a = dot2bf(w3f[i_][0], 0x3f803f80u, c3a); c3b = dot2bf(w3f[i_][1], 0x3f803f80u, c3b); \
        c3a = dot2bf(w3f[i_][2], 0x3f803f80u, c3a); c3b = dot2bf(w3f[i_][3], 0x3f803f80u, c3b); } } while (0)
#define W3L1(i) w3f[i] = __builtin_amdgcn_raw_buffer_load_b128(rs, w3off + (i) * 1024, 0, 16)
#define W3LOAD(lo, hi) do { FENCE(); _Pragma("unroll") for (int i_ = (lo); i_ < (hi); ++i_) \
        w3f[i_] = __builtin_amdgcn_raw_buffer_load_b128(rs, w3off + i_ * 1024, 0, 16); FENCE(); } while (0)
    const u32 img_voff = (lane >> 3) * (D * 2) + ct * 128 + dma_sw * 2;
    LBAR();
    TS(4);
    {
        const int ch = wave >> 2, rq = wave & 3;
        const int r = lane & 15, g = lane >> 4;
        const int o0 = 15 - r + 8 * g;
        const LAS char* Ab = lds + A_R + ch * 4 * RCOPY + (o0 & 3) * RCOPY + (o0 & ~3) * 2 + (6 - rq * 2) * 64;
        const LAS char* Bb = lds + A_U + ch * ULEN * 2 + (16 * r + 8 * g) * 2 + (6 - rq * 2) * 64;
        f32x4 acc[8];
#pragma unroll
        for (int i = 0; i < 8; ++i) acc[i] = f32x4{0, 0, 0, 0};
        u32x4 A0[8], B0[8], A1[8], B1[8];
#define LD1(AF, BF, a, rr) do { \
            u32x2 lo_ = *(const volatile LAS u32x2*)(Ab + (1 - (rr)) * 64 + (7 - (a)) * 512); \
            u32x2 hi_ = *(const volatile LAS u32x2*)(Ab + (1 - (rr)) * 64 + (7 - (a)) * 512 + 8); \
            AF[a] = u32x4{lo_[0], lo_[1], hi_[0], hi_[1]}; \
            BF[a] = *(const LAS u32x4*)(Bb + (1 - (rr)) * 64 + (a) * 512); } while (0)
#define GRP(AF, BF, m) do { _Pragma("unroll") for (int o_ = 0; o_ <= (m); ++o_) if (o_ + (m) <= 7) { \
            acc[o_ + (m)] = mfma16h(AF[m], BF[o_], acc[o_ + (m)]); \
            if (o_ != (m)) acc[o_ + (m)] = mfma16h(AF[o_], BF[m], acc[o_ + (m)]); } } while (0)
        LD1(A0, B0, 0, 0); LD1(A0, B0, 7, 0); LD1(A0, B0, 1, 0); LD1(A0, B0, 6, 0);
        SCHED0();
        GRP(A0, B0, 7); LD1(A0, B0, 2, 0); LD1(A0, B0, 5, 0); SCHED0();
        GRP(A0, B0, 6); LD1(A0, B0, 3, 0); LD1(A0, B0, 4, 0); W3L1(0); SCHED0();
        GRP(A0, B0, 5); W3L1(1); W3L1(2); SCHED0();
        GRP(A0, B0, 4); LD1(A1, B1, 0, 1); W3L1(3); W3L1(4); SCHED0();
        GRP(A0, B0, 3); LD1(A1, B1, 1, 1); W3L1(5); W3L1(6); SCHED0();
        GRP(A0, B0, 2); LD1(A1, B1, 2, 1); W3L1(7); SCHED0();
        GRP(A0, B0, 1); LD1(A1, B1, 3, 1); W3L1(8); SCHED0();
        GRP(A0, B0, 0); GRP(A1, B1, 0); LD1(A1, B1, 4, 1); W3L1(9); SCHED0();
        GRP(A1, B1, 1); LD1(A1, B1, 5, 1); W3L1(10); SCHED0();
        GRP(A1, B1, 2); LD1(A1, B1, 6, 1); W3L1(11); W3L1(12); SCHED0();
        GRP(A1, B1, 3); LD1(A1, B1, 7, 1); W3L1(13); W3L1(14); SCHED0();
        GRP(A1, B1, 4); W3L1(15); W3L1(16); SCHED0();
        GRP(A1, B1, 5); W3L1(17); W3L1(18); SCHED0();
        GRP(A1, B1, 6); W3L1(19); SCHED0();
        GRP(A1, B1, 7);
        LAS char* Yp = lds + A_YP + (rq * 2 + ch) * 8192 + g * 256 + ((r ^ (4 * g) ^ ch) << 4);
#pragma unroll
        for (int c = 0; c < 8; ++c) *(LAS f32x4*)(Yp + 1024 * c) = acc[c];
    }
    LBAR();
    TS(5);
    {
        const int sl = (lane >> 2) ^ (4 * (lane & 3));
        const LAS char* Y0 = lds + A_YP + wave * 1024 + (lane & 3) * 256 + (sl << 4);
        const LAS char* Y1 = lds + A_YP + 8192 + wave * 1024 + (lane & 3) * 256 + ((sl ^ 1) << 4);
        f32x4 y0 = ((*(const LAS f32x4*)(Y0) + *(const LAS f32x4*)(Y0 + 16384)) +
                    (*(const LAS f32x4*)(Y0 + 32768) + *(const LAS f32x4*)(Y0 + 49152))) * inv_scale[0];
        f32x4 y1 = ((*(const LAS f32x4*)(Y1) + *(const LAS f32x4*)(Y1 + 16384)) +
                    (*(const LAS f32x4*)(Y1 + 32768) + *(const LAS f32x4*)(Y1 + 49152))) * inv_scale[1];
        u32 hp[4];
#pragma unroll
        for (int k = 0; k < 4; ++k) {
            f32x2 yy = unpk(pk_bf16(y0[k], y1[k]));
            f32x2 a = unpk(pk_bf16v(yy + unpk(udp[k])));
            hp[k] = pk_bf16v(unpk(xv[k]) + a);
        }
        u32 c00 = __builtin_amdgcn_perm(hp[1], hp[0], 0x05040100), c01 = __builtin_amdgcn_perm(hp[3], hp[2], 0x05040100);
        u32 c10 = __builtin_amdgcn_perm(hp[1], hp[0], 0x07060302), c11 = __builtin_amdgcn_perm(hp[3], hp[2], 0x07060302);
        const bool odd = lane & 1;
        u32 s0 = odd ? c00 : c10, s1 = odd ? c01 : c11;
        u32 r0 = __builtin_amdgcn_update_dpp(0, s0, 0xB1, 0xf, 0xf, true);
        u32 r1 = __builtin_amdgcn_update_dpp(0, s1, 0xB1, 0xf, 0xf, true);
        u32x4 o = odd ? u32x4{r0, r1, c10, c11} : u32x4{c00, c01, r0, r1};
        __builtin_amdgcn_raw_buffer_store_b128(o, rs, WS_HC + (tid >> 4) * 65536 + (d0 + (odd ? 1 : 0)) * 128 + ((tid >> 1) & 7) * 16, 0, 16);
    }
    LBAR();
#pragma unroll
    for (int i = 0; i < 8; ++i) dma_in(srd_w1, img_voff, (wave * 8 + i) * 8 * D * 2, lbase + G_ACT + (wave * 8 + i) * 1024);
    WAIT_V(8);
    LBAR();
    if (tid < 8) __hip_atomic_store((GAS u32*)(ws + WS_F1) + tid * 256 + b, ep, __ATOMIC_RELAXED, __HIP_MEMORY_SCOPE_AGENT);
    TS(6);
#pragma unroll
    for (int i = 0; i < 8; ++i) dma_in(srd_w2, img_voff, (wave * 8 + i) * 8 * D * 2, lbase + A_W + (wave * 8 + i) * 1024);
    WAIT_V(8);
    LBAR();

    const int tq = wave & 1, nq = (wave >> 1) & 1, kh = wave >> 2;
    const int r32 = lane & 31, h32 = lane >> 5;
    const u32 ones = 0x3f803f80u;
    u32x4 wf[32];
    {
        const LAS char* WbE = lds + G_ACT + kh * 8192 + (tr_off ^ (nq << 6)) + nq * 2048;
        const LAS char* WbO = WbE - nq * 4096;
#pragma unroll
        for (int i = 0; i < 16; ++i) wf[i] = tr_frag(((i & 1) ? WbO : WbE) + ((i >> 2) * 8 + (i & 3)) * 2048);
        float ca = 0.f, cb_ = 0.f;
#pragma unroll
        for (int i = 0; i < 16; ++i) {
            ca = dot2bf(wf[i][0], ones, ca); cb_ = dot2bf(wf[i][1], ones, cb_);
            ca = dot2bf(wf[i][2], ones, ca); cb_ = dot2bf(wf[i][3], ones, cb_);
        }
        const float csum = add_xor32(ca + cb_);
        if (h32 == 0 && tq == 0) *(LAS float*)(lds + G_CS + ((nq * 2 + kh) * 32 + r32) * 4) = csum;
    }
    if (wave < 7) CS3(0, 20);
    if (wave == 7) {
        _Pragma("unroll 1") for (int spin = 0; spin < SPIN_MAX; ++spin) {
            FENCE();
            u32x4 f = __builtin_amdgcn_raw_buffer_load_b128(rs, WS_F1 + (b & 7) * 1024 + lane * 16, 0, 16);
            if (__all(f[0] == ep && f[1] == ep && f[2] == ep && f[3] == ep)) break;
            __builtin_amdgcn_s_sleep(1);
        }
        if (b == 0 && lane == 0) *(GAS u32*)(ws + WS_CTL) = ep;
    }
    LBAR();
    TS(7);
    {
        const u32 voff = (lane >> 3) * 128 + dma_sw * 2;
#pragma unroll
        for (int c = 0; c < 4; ++c)
            dma_ws2(srd_ws, voff, WS_HC + tt * 65536 + (8 * c + wave) * 2048, lbase + G_ACT + (8 * c + wave) * 2048);
    }
    float csv[8];
    {
        const LAS char* p = lds + G_CS + nq * 256 + (16 * kh + 4 * h32) * 4;
        f32x4 a0 = *(const LAS f32x4*)(p), a1 = *(const LAS f32x4*)(p + 32);
        f32x4 b0 = *(const LAS f32x4*)(p + 128), b1 = *(const LAS f32x4*)(p + 160);
#pragma unroll
        for (int e = 0; e < 4; ++e) { csv[e] = a0[e] + b0[e]; csv[4 + e] = a1[e] + b1[e]; }
    }
    u32x4 hres, x1res;
    {
        f32x16 acc = {};
        u32x4 own[2];
        float s1a = 0.f, s1b = 0.f, q1a = 0.f, q1b = 0.f;
        const LAS char* Ab = lds + G_ACT + (tr_off ^ (tq << 6));
        const LAS char* AbE = Ab + kh * 8192 + nq * 2048;
        const LAS char* AbO = AbE - nq * 4096;
#pragma unroll
        for (int c = 0; c < 4; ++c) {
            if (c == 0) WAIT_V(6); else if (c == 1) WAIT_V(4); else if (c == 2) WAIT_V(2); else WAIT_V(0);
            LBAR();
#pragma unroll
            for (int kk = 0; kk < 4; ++kk) {
                u32x4 bb = tr_frag(((kk & 1) ? AbO : AbE) + (8 * c + kk) * 2048);
                acc = mfma32(wf[4 * c + kk], bb, acc);
                if ((kk & 1) == 0) own[kk >> 1] = bb;
            }
#pragma unroll
            for (int i = 0; i < 2; ++i) {
                const u32x4 v = own[i];
                s1a = dot2bf(v[0], ones, s1a); s1b = dot2bf(v[1], ones, s1b);
                q1a = dot2bf(v[0], v[0], q1a); q1b = dot2bf(v[1], v[1], q1b);
                s1a = dot2bf(v[2], ones, s1a); s1b = dot2bf(v[3], ones, s1b);
                q1a = dot2bf(v[2], v[2], q1a); q1b = dot2bf(v[3], v[3], q1b);
            }
        }
        hres = tr_frag(Ab + (4 * ct + 2 * nq + kh) * 2048);
        W3LOAD(20, 28);
        TS(8);
        {
            LAS char* p = lds + G_RED + wave * 2048 + lane * 16;
            if (kh == 0) { *(LAS f32x4*)(p) = f32x4{acc[8], acc[9], acc[10], acc[11]}; *(LAS f32x4*)(p + 1024) = f32x4{acc[12], acc[13], acc[14], acc[15]}; }
            else { *(LAS f32x4*)(p) = f32x4{acc[0], acc[1], acc[2], acc[3]}; *(LAS f32x4*)(p + 1024) = f32x4{acc[4], acc[5], acc[6], acc[7]}; }
        }
        float s1 = add_xor32(s1a + s1b), q1 = add_xor32(q1a + q1b);
        LAS char* ps = lds + G_PS + (tq * 32 + r32) * 32;
        if (h32 == 0) *(LAS f32x2*)(ps + (nq * 2 + kh) * 8) = f32x2{s1, q1};
        LBAR();
        TS(9);
        float fin[8];
        {
            const LAS char* p = lds + G_RED + (wave ^ 4) * 2048 + lane * 16;
            f32x4 a0 = *(const LAS f32x4*)(p), a1 = *(const LAS f32x4*)(p + 1024);
#pragma unroll
            for (int e = 0; e < 4; ++e) {
                fin[e] = a0[e] + (kh ? acc[8 + e] : acc[e]);
                fin[4 + e] = a1[e] + (kh ? acc[12 + e] : acc[4 + e]);
            }
        }
        float m1, sd1, m2, sd2;
        f32x4 p0 = *(const LAS f32x4*)(ps), p1 = *(const LAS f32x4*)(ps + 16);
        const float mean = ((p0[0] + p0[2]) + (p1[0] + p1[2])) * (1.0f / 512);
        const float msq = ((p0[1] + p0[3]) + (p1[1] + p1[3])) * (1.0f / 512);
        const float var = fmaxf(msq - mean * mean, 0.f);
        ln_consts(mean, var, m1, sd1);
        const float r1 = __builtin_amdgcn_rcpf(sd1), nb1 = -m1 * r1;
        ln_consts((mean - m1) * r1, var * r1 * r1, m2, sd2);
        const float alpha = r1 * __builtin_amdgcn_rcpf(sd2);
        const float mu = m1 + m2 * sd1;
        const float ga = GC1 * alpha, gb = GC3 * alpha * alpha * alpha;
        u32x4 o;
#pragma unroll
        for (int e = 0; e < 4; ++e) {
            float d0_ = __builtin_fmaf(-mu, csv[2 * e], fin[2 * e]), d1_ = __builtin_fmaf(-mu, csv[2 * e + 1], fin[2 * e + 1]);
            o[e] = pk_bf16(gelu_s(d0_, alpha, ga, gb), gelu_s(d1_, alpha, ga, gb));
        }
        __builtin_amdgcn_raw_buffer_store_b128(o, rs, WS_HG + (((tt * 2 + tq) * 32 + 4 * ct + 2 * nq + kh) * 64 + lane) * 16, 0, 16);
#pragma unroll
        for (int e = 0; e < 4; ++e)
            x1res[e] = pk_bf16(__builtin_fmaf(bf_lo(hres[e]), r1, nb1), __builtin_fmaf(bf_hi(hres[e]), r1, nb1));
    }
    WAIT_V(0);
    LBAR();
    if (tid == 0) __hip_atomic_store((GAS u32*)(ws + WS_F2) + (tt * 8 + ct) * 32, ep, __ATOMIC_RELAXED, __HIP_MEMORY_SCOPE_AGENT);
    TS(10);

    {
        const LAS char* Wb = lds + A_W + kh * 8192 + (tr_off ^ (nq << 6));
#pragma unroll
        for (int i = 0; i < 16; ++i) wf[i] = tr_frag(Wb + ((i >> 2) * 8 + (i & 3)) * 2048);
    }
    if (wave < 7) CS3(20, 28);
    if (wave == 7) {
        const int tl = tt - 1 + (lane >> 3);
        const bool act = lane < 24 && tl >= 0 && tl < 32;
        const GAS u32* fp = (const GAS u32*)(ws + WS_F2) + ((act ? tl : tt) * 8 + (lane & 7)) * 32;
        _Pragma("unroll 1") for (int spin = 0; spin < SPIN_MAX; ++spin) {
            u32 f = __hip_atomic_load(fp, __ATOMIC_RELAXED, __HIP_MEMORY_SCOPE_AGENT);
            if (__all(f == ep)) break;
            __builtin_amdgcn_s_sleep(1);
        }
    }
    LBAR();
    TS(11);
#pragma unroll
    for (int c = 0; c < 4; ++c) {
        const int ks = 8 * c + wave;
        const int sh = c == 0 ? -1 : (c == 1 ? 1 : (c == 2 ? -2 : 2));
#pragma unroll
        for (int i = 0; i < 2; ++i) {
            const int t = tt * 64 + i * 32 + r32 + sh;
            const bool ok = (unsigned)t < (unsigned)L;
            const char* src = ok ? ws + WS_HG + ((((t >> 5) * 32 + ks) * 64) + h32 * 32 + (t & 31)) * 16 : (const char*)g_zero16;
            dma_g(src, lbase + G_ACT + (i * 32 + ks) * 1024);
        }
    }
    {
        f32x16 acc = {};
#pragma unroll
        for (int c = 0; c < 4; ++c) {
            if (c == 0) WAIT_V(6); else if (c == 1) WAIT_V(4); else if (c == 2) WAIT_V(2); else WAIT_V(0);
            LBAR();
#pragma unroll
            for (int kk = 0; kk < 4; ++kk) {
                const int ks = 8 * c + 4 * kh + kk;
                u32x4 bb = *(const LAS u32x4*)(lds + G_ACT + (tq * 32 + ks) * 1024 + lane * 16);
                acc = mfma32(wf[4 * c + kk], bb, acc);
            }
        }
        W3LOAD(28, 32);
        {
            LAS char* p = lds + G_RED + wave * 2048 + lane * 16;
            if (kh == 0) { *(LAS f32x4*)(p) = f32x4{acc[8], acc[9], acc[10], acc[11]}; *(LAS f32x4*)(p + 1024) = f32x4{acc[12], acc[13], acc[14], acc[15]}; }
            else { *(LAS f32x4*)(p) = f32x4{acc[0], acc[1], acc[2], acc[3]}; *(LAS f32x4*)(p + 1024) = f32x4{acc[4], acc[5], acc[6], acc[7]}; }
        }
        LBAR();
        TS(12);
        const LAS char* p = lds + G_RED + (wave ^ 4) * 2048 + lane * 16;
        f32x4 a0 = *(const LAS f32x4*)(p), a1 = *(const LAS f32x4*)(p + 1024);
        float fin[8];
#pragma unroll
        for (int e = 0; e < 4; ++e) {
            fin[e] = a0[e] + (kh ? acc[8 + e] : acc[e]);
            fin[4 + e] = a1[e] + (kh ? acc[12 + e] : acc[4 + e]);
        }
#pragma unroll
        for (int e = 0; e < 4; ++e)
            x1res[e] = pk_bf16v(unpk(x1res[e]) + unpk(pk_bf16(fin[2 * e], fin[2 * e + 1])));
        __builtin_amdgcn_raw_buffer_store_b128(x1res, rs, WS_X1 + (((tt * 2 + tq) * 32 + 4 * ct + 2 * nq + kh) * 64 + lane) * 16, 0, 16);
        {
            float sa = 0.f, qa = 0.f, sb = 0.f, qb = 0.f;
#pragma unroll
            for (int e = 0; e < 4; ++e) {
                const float lo = bf_lo(x1res[e]), hi = bf_hi(x1res[e]);
                sa += lo; sb += hi; qa = __builtin_fmaf(lo, lo, qa); qb = __builtin_fmaf(hi, hi, qb);
            }
            const float st = add_xor32(sa + sb), qt = add_xor32(qa + qb);
            if (h32 == 0)
                __builtin_amdgcn_raw_buffer_store_b64(u32x2{__float_as_uint(st), __float_as_uint(qt)}, rs,
                                                      WS_PST + ((tt * 2 + tq) * 32 + ct * 4 + nq * 2 + kh) * 256 + r32 * 8, 0, 16);
        }
        *(LAS u32x4*)(lds + G_SPARE + wave * 2048 + lane * 16) = x1res;
        *(LAS u32x4*)(lds + G_SPARE + wave * 2048 + 1024 + lane * 16) = hres;
    }
    WAIT_V(0);
    LBAR();
    if (tid == 0) __hip_atomic_store((GAS u32*)(ws + WS_F3) + (tt * 8 + ct) * 32, ep, __ATOMIC_RELAXED, __HIP_MEMORY_SCOPE_AGENT);
    TS(13);
    const int kq = 2 * tq + kh;
    const u32 w4base = WS_WT4 + ((ct * 2 + nq) * 112 + kq * 28) * 1024;
    const u32 w4st = wave < 2 ? 65536 + wave * 8192 : (wave < 4 ? 81920 + wave * 8192 : 98304 + wave * 8192);
    dma_ws4(srd_ws, lane * 16, w4base + 20 * 1024, lbase + w4st);
    dma_ws4(srd_ws, lane * 16, w4base + 24 * 1024, lbase + w4st + 4096);

    const u32 w4off = w4base + lane * 16;
    u32x4 w4f[28];
    const GAS u32* f4p = (const GAS u32*)(ws + WS_F4) + (tt * 8 + (lane & 7)) * 32;
#define ACT3_DMA() do { \
        dma_ws4(srd_ws, lane * 16, WS_X1 + tt * 65536 + wave * 4096, lbase + G_ACT + wave * 4096); \
        dma_ws4(srd_ws, lane * 16, WS_X1 + tt * 65536 + 32768 + wave * 4096, lbase + G_ACT + 32768 + wave * 4096); } while (0)
#define POLL(ptr) do { _Pragma("unroll 1") for (int spin_ = 0; spin_ < SPIN_MAX; ++spin_) { \
        u32 f_ = __hip_atomic_load((ptr), __ATOMIC_RELAXED, __HIP_MEMORY_SCOPE_AGENT); \
        if (__all(f_ == ep)) break; \
        __builtin_amdgcn_s_sleep(1); } } while (0)
    if (wave == 7) {
        if (lane == 0) *(LAS u32*)(lds + G_CS + 2048) = 0u;
        POLL((const GAS u32*)(ws + WS_F3) + (tt * 8 + (lane & 7)) * 32);
        LBAR();
        ACT3_DMA();
        u32x2 pp[2][16];
#pragma unroll
        for (int i = 0; i < 2; ++i)
#pragma unroll
            for (int j = 0; j < 16; ++j)
                pp[i][j] = __builtin_amdgcn_raw_buffer_load_b64(rs, WS_PST + ((tt * 2 + i) * 32 + h32 * 16 + j) * 256 + r32 * 8, 0, 16);
        FENCE();
        WAIT_V(36);
        LBAR();
#pragma unroll
        for (int i = 0; i < 2; ++i) {
            float sa = 0.f, qa = 0.f, sb = 0.f, qb = 0.f;
#pragma unroll
            for (int j = 0; j < 16; j += 2) {
                sa += __uint_as_float(pp[i][j][0]); qa += __uint_as_float(pp[i][j][1]);
                sb += __uint_as_float(pp[i][j + 1][0]); qb += __uint_as_float(pp[i][j + 1][1]);
            }
            float mean = add_xor32(sa + sb) * (1.0f / 512), msq = add_xor32(qa + qb) * (1.0f / 512);
            float m, sd;
            ln_consts(mean, fmaxf(msq - mean * mean, 0.f), m, sd);
            if (h32 == 0) *(LAS f32x2*)(lds + G_PS + (i * 32 + r32) * 8) = f32x2{__builtin_amdgcn_rcpf(sd), m};
        }
        FENCE();
#pragma unroll
        for (int j = 0; j < 10; ++j) { w4f[j] = __builtin_amdgcn_raw_buffer_load_b128(rs, w4off + j * 1024, 0, 16); __builtin_amdgcn_s_sleep(2); }
        FENCE();
        LBAR();
#pragma unroll
        for (int j = 10; j < 20; ++j) { w4f[j] = __builtin_amdgcn_raw_buffer_load_b128(rs, w4off + j * 1024, 0, 16); __builtin_amdgcn_s_sleep(2); }
        FENCE();
#pragma unroll
        for (int j = 0; j < 8; ++j) w4f[20 + j] = *(const LAS u32x4*)(lds + w4st + j * 1024 + lane * 16);
        LBAR();
        POLL(f4p);
#pragma unroll
        for (int i = 0; i < 28; ++i) PIN4(w4f[i]);
        LBAR();
    } else {
        CS3(28, 32);
        {
            const float csum = add_xor32(c3a + c3b);
            if (h32 == 0) *(LAS float*)(lds + G_CS + (wave * 32 + r32) * 4) = csum;
        }
        LBAR();
        TS(14);
        ACT3_DMA();
        const LAS char* Ab = lds + G_ACT + lane * 16;
        const u32 gdst = WS_G + ((tt * 2 * 112 + (ct * 7 + wave) * 2) * 64 + lane) * 16;
        f32x16 acc0 = {}, acc1 = {};
        u32x4 bq[4];
        WAIT_V(4);
        LBAR();
        TS(30);
#pragma unroll
        for (int i = 0; i < 4; ++i) bq[i] = *(const LAS u32x4*)(Ab + i * 1024);
#pragma unroll
        for (int ks = 0; ks < 32; ++ks) {
            acc0 = mfma32(w3f[ks], bq[ks & 3], acc0);
            if (ks + 4 < 32) bq[ks & 3] = *(const LAS u32x4*)(Ab + (ks + 4) * 1024);
            if (ks % 3 == 0) w4f[ks / 3] = __builtin_amdgcn_raw_buffer_load_b128(rs, w4off + (ks / 3) * 1024, 0, 16);
            SCHED0();
        }
        TS(31);
        WAIT_V(11);
        LBAR();
        TS(15);
#pragma unroll
        for (int i = 0; i < 4; ++i) bq[i] = *(const LAS u32x4*)(Ab + (32 + i) * 1024);
        float cs[16];
#pragma unroll
        for (int i = 0; i < 4; ++i) {
            f32x4 c4 = *(const LAS f32x4*)(lds + G_CS + (wave * 32 + 8 * i + 4 * h32) * 4);
            cs[4 * i] = c4[0]; cs[4 * i + 1] = c4[1]; cs[4 * i + 2] = c4[2]; cs[4 * i + 3] = c4[3];
        }
        {
            const f32x2 st = *(const LAS f32x2*)(lds + G_PS + r32 * 8);
            const float ga = GC1 * st.x, gb = GC3 * st.x * st.x * st.x;
            float g[16];
            SCHED0();
#pragma unroll
            for (int k4 = 0; k4 < 32; k4 += 4) {
#pragma unroll
                for (int ks = k4; ks < k4 + 4; ++ks) {
                    acc1 = mfma32(w3f[ks], bq[ks & 3], acc1);
                    if (ks + 4 < 32) bq[ks & 3] = *(const LAS u32x4*)(Ab + (32 + ks + 4) * 1024);
                    if (ks < 16) g[ks] = gelu_s(__builtin_fmaf(-st.y, cs[ks], acc0[ks]), st.x, ga, gb);
                    if (ks >= 20 && ks < 29) w4f[11 + ks - 20] = __builtin_amdgcn_raw_buffer_load_b128(rs, w4off + (11 + ks - 20) * 1024, 0, 16);
                    if (ks >= 24) w4f[20 + ks - 24] = *(const LAS u32x4*)(lds + w4st + (ks - 24) * 1024 + lane * 16);
                }
                if (k4 == 8 || k4 == 16) {
                    const int o = k4 - 8;
                    u32x4 v = {pk_bf16(g[o], g[o + 1]), pk_bf16(g[o + 2], g[o + 3]), pk_bf16(g[o + 4], g[o + 5]), pk_bf16(g[o + 6], g[o + 7])};
                    __builtin_amdgcn_raw_buffer_store_b128(v, rs, gdst + (o >> 3) * 1024, 0, 16);
                }
#pragma unroll
                for (int q = 0; q < 4; ++q) {
                    __builtin_amdgcn_sched_group_barrier(0x008, 1, 0);
                    __builtin_amdgcn_sched_group_barrier(0x100, 1, 0);
                    __builtin_amdgcn_sched_group_barrier(0x002, 10, 0);
                    __builtin_amdgcn_sched_group_barrier(0x010, 1, 0);
                }
                SCHED0();
            }
        }
        WAIT_V(9);
        LBAR();
        if (tid == 0) __hip_atomic_store((GAS u32*)(ws + WS_F4) + (tt * 8 + ct) * 32, ep, __ATOMIC_RELAXED, __HIP_MEMORY_SCOPE_AGENT);
        TS(16);
        {
            const f32x2 st = *(const LAS f32x2*)(lds + G_PS + (32 + r32) * 8);
            const float ga = GC1 * st.x, gb = GC3 * st.x * st.x * st.x;
#pragma unroll
            for (int sI = 0; sI < 2; ++sI) {
                u32x4 v;
#pragma unroll
                for (int e = 0; e < 4; ++e) {
                    const int j = 8 * sI + 2 * e;
                    v[e] = pk_bf16(gelu_s(__builtin_fmaf(-st.y, cs[j], acc1[j]), st.x, ga, gb),
                                   gelu_s(__builtin_fmaf(-st.y, cs[j + 1], acc1[j + 1]), st.x, ga, gb));
                }
                __builtin_amdgcn_raw_buffer_store_b128(v, rs, gdst + 112 * 1024 + sI * 1024, 0, 16);
            }
        }
        TS(17);
        WAIT_V(0);
        if (lane == 0) {
            const u32 old = __hip_atomic_fetch_add((LAS u32*)(lds + G_CS + 2048), 1u, __ATOMIC_RELAXED, __HIP_MEMORY_SCOPE_WORKGROUP);
            if (old == 6) __hip_atomic_store((GAS u32*)(ws + WS_F5) + (tt * 8 + ct) * 32, ep, __ATOMIC_RELAXED, __HIP_MEMORY_SCOPE_AGENT);
        }
#pragma unroll
        for (int i = 0; i < 28; ++i) PIN4(w4f[i]);
        LBAR();
    }
    TS(18);
    {
        const u32 gsrc = WS_G + (tt * 2 * 112 + wave * 14) * 1024;
        const u32 ldst = lbase + wave * 14336;
#define FILL_A(t) do { dma_ws4(srd_ws, lane * 16, gsrc + (t) * 114688, ldst); dma_ws4(srd_ws, lane * 16, gsrc + (t) * 114688 + 4096, ldst + 4096); } while (0)
#define FILL_B(t, dst) do { dma_ws4(srd_ws, lane * 16, gsrc + (t) * 114688 + 8192, (dst)); dma_ws2(srd_ws, lane * 16, gsrc + (t) * 114688 + 12288, (dst) + 4096); } while (0)
        FILL_A(0); FILL_B(0, ldst + 8192);
        x1res = *(const LAS u32x4*)(lds + G_SPARE + wave * 2048 + lane * 16);
        hres = *(const LAS u32x4*)(lds + G_SPARE + wave * 2048 + 1024 + lane * 16);
        const LAS char* Gb = lds + kq * 28672 + lane * 16;
        const LAS char* Gs = lds + G_SPARE + kq * 12288 + lane * 16;
        f32x16 acc0 = {}, acc1 = {};
        u32x4 bq[4];
#define KA(i) ((i) < 8 ? (i) : (i) + 6)
#define KB(i) ((i) < 6 ? (i) + 8 : (i) + 16)
#define KS(i) (i)
#define PART(ACC, BASE, KF, KW, N) do { \
        _Pragma("unroll") for (int i_ = 0; i_ < 4; ++i_) bq[i_] = *(const LAS u32x4*)(BASE + KF(i_) * 1024); \
        _Pragma("unroll") for (int i_ = 0; i_ < N; ++i_) { \
            ACC = mfma32(w4f[KW(i_)], bq[i_ & 3], ACC); \
            if (i_ + 4 < N) bq[i_ & 3] = *(const LAS u32x4*)(BASE + KF(i_ + 4) * 1024); \
            SCHED0(); } } while (0)
        if (wave == 7) POLL(f4p + 8192);
        WAIT_V(6);
        LBAR();
        TS(20);
        {
#pragma unroll
            for (int i_ = 0; i_ < 4; ++i_) bq[i_] = *(const LAS u32x4*)(Gb + KA(i_) * 1024);
#pragma unroll
            for (int i_ = 0; i_ < 16; ++i_) {
                if (i_ % 4 == 1 && i_ < 12) dma_ws2(srd_ws, lane * 16, gsrc + 114688 + 8192 + (i_ / 4) * 2048, lbase + G_SPARE + wave * 6144 + (i_ / 4) * 2048);
                acc0 = mfma32(w4f[KA(i_)], bq[i_ & 3], acc0);
                if (i_ + 4 < 16) bq[i_ & 3] = *(const LAS u32x4*)(Gb + KA(i_ + 4) * 1024);
                SCHED0();
            }
        }
        WAIT_V(6);
        LBAR();
        TS(21);
        {
#pragma unroll
            for (int i_ = 0; i_ < 4; ++i_) bq[i_] = *(const LAS u32x4*)(Gb + KB(i_) * 1024);
#pragma unroll
            for (int i_ = 0; i_ < 12; ++i_) {
                if (i_ < 4) dma_ws2(srd_ws, lane * 16, gsrc + 114688 + i_ * 2048, ldst + i_ * 2048);
                acc0 = mfma32(w4f[KB(i_)], bq[i_ & 3], acc0);
                if (i_ + 4 < 12) bq[i_ & 3] = *(const LAS u32x4*)(Gb + KB(i_ + 4) * 1024);
                SCHED0();
            }
        }
        WAIT_V(8);
        LBAR();
        TS(22);
        PART(acc1, Gs, KS, KB, 12);
        WAIT_V(0);
        LBAR();
        TS(23);
        PART(acc1, Gb, KA, KA, 16);
        TS(24);
#pragma unroll
        for (int p = 0; p < 4; ++p) {
            if (p != kq) {
                const int slot = kq > p ? kq - 1 : kq;
                LAS char* dst = lds + G_SPARE + ((nq * 4 + p) * 3 + slot) * 2048 + lane * 16;
                const int o = 8 * (p & 1);
                if (p < 2) { *(LAS f32x4*)(dst) = f32x4{acc0[o], acc0[o + 1], acc0[o + 2], acc0[o + 3]};
                             *(LAS f32x4*)(dst + 1024) = f32x4{acc0[o + 4], acc0[o + 5], acc0[o + 6], acc0[o + 7]}; }
                else { *(LAS f32x4*)(dst) = f32x4{acc1[o], acc1[o + 1], acc1[o + 2], acc1[o + 3]};
                       *(LAS f32x4*)(dst + 1024) = f32x4{acc1[o + 4], acc1[o + 5], acc1[o + 6], acc1[o + 7]}; }
            }
        }
        float fin[8];
#pragma unroll
        for (int e = 0; e < 8; ++e) {
            float lo = tq ? acc1[e] : acc0[e], hi = tq ? acc1[8 + e] : acc0[8 + e];
            fin[e] = kh ? hi : lo;
        }
        PIN4(hres); PIN4(x1res);
        LBAR();
#pragma unroll
        for (int sl = 0; sl < 3; ++sl) {
            const LAS char* src = lds + G_SPARE + ((nq * 4 + kq) * 3 + sl) * 2048 + lane * 16;
            f32x4 a0 = *(const LAS f32x4*)(src), a1 = *(const LAS f32x4*)(src + 1024);
#pragma unroll
            for (int e = 0; e < 4; ++e) { fin[e] += a0[e]; fin[4 + e] += a1[e]; }
        }
        u32 o[4];
#pragma unroll
        for (int e = 0; e < 4; ++e) {
            f32x2 h2 = unpk(pk_bf16(fin[2 * e], fin[2 * e + 1]));
            f32x2 t1 = unpk(pk_bf16v(unpk(x1res[e]) + h2));
            o[e] = pk_bf16v(unpk(hres[e]) + t1);
        }
        int tz = threadIdx.x;
        asm volatile("" : "+v"(tz));
        u16* orow = out + (size_t)(tt * 64 + tq * 32 + (tz & 31)) * D + ct * 64 + nq * 32 + kh * 16 + 4 * ((tz >> 5) & 1);
        *(u32x2*)(orow) = u32x2{o[0], o[1]};
        *(u32x2*)(orow + 8) = u32x2{o[2], o[3]};
    }
    TS(25);
}

extern "C" void kernel_launch(void* const* d_in, const int* in_sizes, int n_in, void* d_out, int out_size,
                              void* d_ws, size_t ws_size, hipStream_t stream) {
    (void)hipFuncSetAttribute((const void*)k_mega, hipFuncAttributeMaxDynamicSharedMemorySize, LDS_BYTES);
    k_mega<<<256, 512, LDS_BYTES, stream>>>((const u16*)d_in[0], (const u16*)d_in[1], (const u16*)d_in[2],
                                            (const u16*)d_in[3], (const u16*)d_in[4], (const u16*)d_in[5],
                                            (const u16*)d_in[6], (const u16*)d_in[7], (const u16*)d_in[8],
                                            (const u16*)d_in[10], (const u16*)d_in[11], (const u16*)d_in[12],
                                            (const u16*)d_in[13], (u16*)d_out, (char*)d_ws + ((ws_size - WS_END) & ~(size_t)0x1FFFFF));
}
```

```cpp
#define CWARM

#include <hip/hip_runtime.h>
#include <hip/hip_bf16.h>
#include <stdint.h>

#define DEV __device__ __forceinline__
#define LAS __attribute__((address_space(3)))
#define GAS __attribute__((address_space(1)))
typedef unsigned int u32;
typedef unsigned short u16;
typedef unsigned long long u64;
typedef float f32x2 __attribute__((ext_vector_type(2)));
typedef float f32x4 __attribute__((ext_vector_type(4)));
typedef float f32x16 __attribute__((ext_vector_type(16)));
typedef u32 u32x2 __attribute__((ext_vector_type(2)));
typedef u32 u32x4 __attribute__((ext_vector_type(4)));
typedef short s16x4 __attribute__((ext_vector_type(4)));
typedef __bf16 bf16x2 __attribute__((ext_vector_type(2)));
typedef __bf16 bf16x8 __attribute__((ext_vector_type(8)));
typedef _Float16 f16x2 __attribute__((ext_vector_type(2)));
typedef _Float16 f16x8 __attribute__((ext_vector_type(8)));

#define TS(i)

constexpr int L = 2048, D = 512, NS = 128, F = 1792;

constexpr u32 WS_CTL = 0;
constexpr u32 WS_F1 = 8192;
constexpr u32 WS_F0 = 16384;
constexpr u32 WS_F2 = 32768;
constexpr u32 WS_F3 = 65536;
constexpr u32 WS_F4 = 98304;
constexpr u32 WS_F5 = 131072;
constexpr u32 WS_WT3 = 163840;
constexpr u32 WS_WT4 = WS_WT3 + 512 * 1792 * 2;
constexpr u32 WS_HC = WS_WT4 + 512 * 1792 * 2;
constexpr u32 WS_HG = WS_HC + 2048 * 512 * 2;
constexpr u32 WS_X1 = WS_HG + 2048 * 512 * 2;
constexpr u32 WS_G = WS_X1 + 2048 * 512 * 2;
constexpr u32 WS_XU = WS_G + 2048 * 1792 * 2;
constexpr u32 WS_PST = WS_XU + 16384;
constexpr u32 WS_END = WS_PST + 64 * 8192;

__device__ const u32x4 g_zero16[4] = {};

constexpr int c_round_bf16(int Lg) {
    if (Lg < 256) return Lg;
    int e = 0;
    while ((Lg >> (e + 1)) != 0) ++e;
    int sh = e - 7, q = Lg >> sh, rem = Lg & ((1 << sh) - 1), half = 1 << (sh - 1);
    if (rem > half || (rem == half && (q & 1))) ++q;
    return q << sh;
}
constexpr int c_ci(int Lr) {
    if (Lr < 256) return Lr;
    int e = 0;
    while ((Lr >> (e + 1)) != 0) ++e;
    return 128 * (e - 6) + ((Lr >> (e - 7)) - 128);
}
constexpr int KC_ZERO = 644, KC_DUMMY = 648, KC_LEN = 656;
struct Tabs {
    u16 ci[3 * 64 * 4];
    u16 ex[256 * 12];
    constexpr Tabs() : ci(), ex() {
        for (int mt = 0; mt < 3; ++mt)
            for (int l = 0; l < 64; ++l)
                for (int e = 0; e < 4; ++e) {
                    int row = mt * 16 + (l >> 4) * 4 + e, j = l & 15, lrow = 0;
                    if (row < 16) lrow = (row & 7) + 128 * (row >> 3);
                    else if (row < 24) lrow = 256 + 2 * (row & 3) + 128 * ((row >> 2) & 1);
                    else if (row < 32) lrow = 512 + 4 * (row & 1) + 128 * ((row >> 1) & 3);
                    else if (row < 40) lrow = 1024 + 128 * (row & 7);
                    else lrow = 2048;
                    bool valid = row < 40 || (row == 40 && j == 0);
                    ci[(mt * 64 + l) * 4 + e] = (u16)(2 * (valid ? c_ci(lrow + 8 * j) : KC_DUMMY));
                }
        for (int c = 0; c < 256; ++c)
            for (int i = 0; i < 12; ++i) {
                int lag = 2047 - (8 * c + i);
                ex[c * 12 + i] = (u16)(2 * (lag < 0 ? KC_ZERO : c_ci(c_round_bf16(lag))));
            }
    }
};
__device__ const Tabs g_tabs = Tabs();

DEV float bf_lo(u32 v) { return __uint_as_float(v << 16); }
DEV float bf_hi(u32 v) { return __uint_as_float(v & 0xffff0000u); }
DEV float bf1(u16 v) { return __uint_as_float(((u32)v) << 16); }
DEV u32 pk_bf16(float a, float b) {
    f32x2 f = {a, b};
    bf16x2 r = __builtin_convertvector(f, bf16x2);
    return __builtin_bit_cast(u32, r);
}
DEV u32 pk_bf16v(f32x2 f) {
    bf16x2 r = __builtin_convertvector(f, bf16x2);
    return __builtin_bit_cast(u32, r);
}
DEV f32x2 unpk(u32 v) { return f32x2{bf_lo(v), bf_hi(v)}; }
DEV float rbf(float a) { return bf_lo(pk_bf16(a, 0.f)); }
DEV u32 pk_f16(float a, float b) {
    f32x2 f = {a, b};
    f16x2 r = __builtin_convertvector(f, f16x2);
    return __builtin_bit_cast(u32, r);
}
DEV float dot2bf(u32 a, u32 b, float c) {
    return __builtin_amdgcn_fdot2_f32_bf16(__builtin_bit_cast(bf16x2, a), __builtin_bit_cast(bf16x2, b), c, false);
}
DEV f32x16 mfma32(u32x4 a, u32x4 b, f32x16 c) {
    return __builtin_amdgcn_mfma_f32_32x32x16_bf16(__builtin_bit_cast(bf16x8, a), __builtin_bit_cast(bf16x8, b), c, 0, 0, 0);
}
DEV f32x4 mfma16h(u32x4 a, u32x4 b, f32x4 c) {
    return __builtin_amdgcn_mfma_f32_16x16x32_f16(__builtin_bit_cast(f16x8, a), __builtin_bit_cast(f16x8, b), c, 0, 0, 0);
}
DEV f32x2 gelu2(f32x2 x) {
    const float c1 = -2.0f * 0.7978845608028654f * 1.4426950408889634f;
    const float c3 = c1 * 0.044715f;
    f32x2 t = x * (x * x * c3 + c1);
    f32x2 e = {__builtin_amdgcn_exp2f(t.x), __builtin_amdgcn_exp2f(t.y)};
    e = e + 1.0f;
    f32x2 r = {__builtin_amdgcn_rcpf(e.x), __builtin_amdgcn_rcpf(e.y)};
    return x * r;
}
template <int CTRL, int RM>
DEV float dpp_f(float v) {
    return __int_as_float(__builtin_amdgcn_update_dpp(0, __float_as_int(v), CTRL, RM, 0xf, true));
}
DEV float wave_sum(float v) {
    v += dpp_f<0x111, 0xf>(v); v += dpp_f<0x112, 0xf>(v); v += dpp_f<0x114, 0xf>(v); v += dpp_f<0x118, 0xf>(v);
    v += dpp_f<0x142, 0xa>(v); v += dpp_f<0x143, 0xc>(v);
    return __int_as_float(__builtin_amdgcn_readlane(__float_as_int(v), 63));
}
DEV float wave_max(float v) {
    v = fmaxf(v, dpp_f<0x111, 0xf>(v)); v = fmaxf(v, dpp_f<0x112, 0xf>(v)); v = fmaxf(v, dpp_f<0x114, 0xf>(v));
    v = fmaxf(v, dpp_f<0x118, 0xf>(v)); v = fmaxf(v, dpp_f<0x142, 0xa>(v)); v = fmaxf(v, dpp_f<0x143, 0xc>(v));
    return __int_as_float(__builtin_amdgcn_readlane(__float_as_int(v), 63));
}
DEV float add_xor32(float v) {
    u32 u = __float_as_uint(v);
    auto sw = __builtin_amdgcn_permlane32_swap(u, u, false, false);
    u32 a = sw[0], b = sw[1];
    return __uint_as_float(a) + __uint_as_float(b);
}
#define WAIT_V(n) asm volatile("s_waitcnt vmcnt(" #n ")" ::: "memory")
#define FENCE() asm volatile("" ::: "memory")
#define LBAR() do { asm volatile("s_waitcnt lgkmcnt(0)" ::: "memory"); __builtin_amdgcn_s_barrier(); asm volatile("" ::: "memory"); } while (0)
#define PIN4(a) asm volatile("" : "+v"(a))

DEV void ln_consts(float mean, float var, float& m_bf, float& sd_bf) {
    m_bf = rbf(mean);
    float v = rbf(var);
    v = rbf(v + 1e-5f);
    sd_bf = rbf(__builtin_amdgcn_sqrtf(v));
}
DEV void dma_ws(u32x4 srd, u32 voff, u32 soff, u32 ldst) {
    asm volatile("s_mov_b32 m0, %0\n\ts_nop 0\n\tbuffer_load_dwordx4 %1, %2, %3 offen sc1 lds"
                 :: "s"(ldst), "v"(voff), "s"(srd), "s"(soff) : "memory");
}
DEV void dma_ws2(u32x4 srd, u32 voff, u32 soff, u32 ldst) {
    asm volatile("s_mov_b32 m0, %0\n\ts_nop 0\n\tbuffer_load_dwordx4 %1, %2, %3 offen sc1 lds\n\t"
                 "buffer_load_dwordx4 %1, %2, %3 offen offset:1024 sc1 lds"
                 :: "s"(ldst), "v"(voff), "s"(srd), "s"(soff) : "memory");
}
DEV void dma_ws4(u32x4 srd, u32 voff, u32 soff, u32 ldst) {
    asm volatile("s_mov_b32 m0, %0\n\ts_nop 0\n\tbuffer_load_dwordx4 %1, %2, %3 offen sc1 lds\n\t"
                 "buffer_load_dwordx4 %1, %2, %3 offen offset:1024 sc1 lds\n\t"
                 "buffer_load_dwordx4 %1, %2, %3 offen offset:2048 sc1 lds\n\t"
                 "buffer_load_dwordx4 %1, %2, %3 offen offset:3072 sc1 lds"
                 :: "s"(ldst), "v"(voff), "s"(srd), "s"(soff) : "memory");
}
DEV void dma_in(u32x4 srd, u32 voff, u32 soff, u32 ldst) {
    asm volatile("s_mov_b32 m0, %0\n\ts_nop 0\n\tbuffer_load_dwordx4 %1, %2, %3 offen lds"
                 :: "s"(ldst), "v"(voff), "s"(srd), "s"(soff) : "memory");
}
DEV void dma_g(const void* g, u32 ldst) {
    asm volatile("s_mov_b32 m0, %0\n\ts_nop 0\n\tglobal_load_lds_dwordx4 %1, off sc1"
                 :: "s"(ldst), "v"(g) : "memory");
}
DEV u32x4 make_srd(const void* p, u32 bytes) {
    u64 a = (u64)p;
    return u32x4{(u32)a, (u32)(a >> 32) & 0xffffu, bytes, 0x00020000u};
}
#define SCHED0() __builtin_amdgcn_sched_barrier(0)
DEV u32x4 tr_frag(const LAS char* p) {
    s16x4 a = __builtin_amdgcn_ds_read_tr16_b64_v4i16((LAS s16x4*)p);
    s16x4 b = __builtin_amdgcn_ds_read_tr16_b64_v4i16((LAS s16x4*)(p + 1024));
    u32x2 x = __builtin_bit_cast(u32x2, a), y = __builtin_bit_cast(u32x2, b);
    return u32x4{x[0], x[1], y[0], y[1]};
}

struct cplx { float x, y; };
DEV cplx cmul(cplx a, cplx b) {
    cplx r;
    asm("v_mul_f32 %0, %3, %5\n\tv_mul_f32 %1, %3, %4\n\tv_fma_f32 %0, %2, %4, -%0\n\tv_fma_f32 %1, %2, %5, %1"
        : "=&v"(r.x), "=&v"(r.y) : "v"(a.x), "v"(a.y), "v"(b.x), "v"(b.y));
    return r;
}
DEV cplx csq(cplx a) {
    cplx r;
    asm("v_mul_f32 %0, %3, %3\n\tv_mul_f32 %1, %2, %3\n\tv_fma_f32 %0, %2, %2, -%0\n\tv_add_f32 %1, %1, %1"
        : "=&v"(r.x), "=&v"(r.y) : "v"(a.x), "v"(a.y));
    return r;
}
DEV cplx cexp_pow2(float tr, float wh, float wl, float p2) {
    float mag = __builtin_amdgcn_exp2f(tr * p2);
    float ph = __builtin_amdgcn_fractf(wh * p2) + wl * p2;
    return cplx{mag * __builtin_amdgcn_cosf(ph), mag * __builtin_amdgcn_sinf(ph)};
}
template <int CTRL, int RM>
DEV u32 dpp_u(u32 v) { return (u32)__builtin_amdgcn_update_dpp(0, (int)v, CTRL, RM, 0xf, true); }
DEV u32 wave_max_u(u32 v) {
    v = max(v, dpp_u<0x111, 0xf>(v)); v = max(v, dpp_u<0x112, 0xf>(v)); v = max(v, dpp_u<0x114, 0xf>(v));
    v = max(v, dpp_u<0x118, 0xf>(v)); v = max(v, dpp_u<0x142, 0xa>(v)); v = max(v, dpp_u<0x143, 0xc>(v));
    return (u32)__builtin_amdgcn_readlane((int)v, 63);
}
DEV float gelu_s(float d, float r, float ga, float gb) {
    float t = d * __builtin_fmaf(d * d, gb, ga);
    float e = __builtin_amdgcn_exp2f(t);
    return (d * r) * __builtin_amdgcn_rcpf(1.0f + e);
}
constexpr float GC1 = -2.0f * 0.7978845608028654f * 1.4426950408889634f, GC3 = GC1 * 0.044715f;

constexpr int TRS = 528;
constexpr int RCOPY = 4160;
constexpr int A_R = 0;
constexpr int A_U = 33280, UPAD = 240, ULEN = 2304;
constexpr int A_KC = A_U + 2 * ULEN * 2;
constexpr int A_X = 45312, A_XCH = 41 * TRS;
constexpr int A_Y = A_X + 2 * A_XCH, A_YCH = 16 * TRS;
constexpr int A_SCR = 107520;
constexpr int A_YP = 45312;
constexpr int A_W = 98304;
constexpr int A_XI = 123904, XI_RS = 1088;
constexpr int LDS_BYTES = 163840;
constexpr int G_ACT = 0, G_RED = 65536, G_PS = 81920, G_CS = 86016, G_SPARE = 114688;
constexpr int SPIN_MAX = 1 << 18;

__global__ __launch_bounds__(512, 2) void k_mega(const u16* __restrict__ x, const u16* __restrict__ A_re,
                                                 const u16* __restrict__ A_im, const u16* __restrict__ B_re,
                                                 const u16* __restrict__ B_im, const u16* __restrict__ C_re,
                                                 const u16* __restrict__ C_im, const u16* __restrict__ log_dt,
                                                 const u16* __restrict__ Dskip, const u16* __restrict__ w1,
                                                 const u16* __restrict__ w2, const u16* __restrict__ w3,
                                                 const u16* __restrict__ w4, u16* __restrict__ out, char* ws) {
    extern __shared__ __attribute__((aligned(16))) char lds_raw[];
    LAS char* lds = (LAS char*)lds_raw;
    const int tid = threadIdx.x, lane = tid & 63, wave = __builtin_amdgcn_readfirstlane(tid >> 6);
    const int b = blockIdx.x, bq = b >> 3;
    const int cb = (b & 7) * 32 + bq, d0 = cb * 2;
    const int tt = (b & 7) * 4 + (bq >> 3), ct = bq & 7;
    const auto rs = __builtin_amdgcn_make_buffer_rsrc(ws, 0, WS_END, 0x00020000);
    const u32x4 srd_ws = make_srd(ws, WS_END), srd_w1 = make_srd(w1, D * D * 2), srd_w2 = make_srd(w2, D * D * 2);
    const u32 lbase = (u32)(size_t)lds;
    asm volatile("" :: "s"(x), "s"(A_re), "s"(A_im), "s"(B_re), "s"(B_im), "s"(C_re), "s"(C_im), "s"(log_dt), "s"(Dskip),
                 "s"(w1), "s"(w2), "s"(w3), "s"(w4), "s"(out), "s"(ws));
    u32 cw0 = 0;
    {
        u64 pc; u32 clen;
        asm volatile("s_getpc_b64 %0\n.Lkm_begin:\n\ts_mov_b32 %1, .Lkm_end-.Lkm_begin" : "=s"(pc), "=s"(clen));
        const u32 off = ((bq & 15) + 16 * lane) * 128;
        const char* cp = (const char*)pc + off;
        if (wave == 0 && off + 4 <= clen) asm volatile("global_load_dword %0, %1, off" : "=v"(cw0) : "v"(cp) : "memory");
    }
    TS(0);

    const u32 ep = *(const GAS u32*)(ws + WS_CTL) + 1u;
    u32x4 xr = *(const u32x4*)(x + (size_t)(b * 8 + wave) * D + lane * 8);
    const int kch = wave >> 2, knh = wave & 1, kpart = (wave >> 1) & 1;
    const int pidx = (d0 + kch) * NS + knh * 64 + lane;
    u16 p_are = A_re[pidx], p_aim = A_im[pidx], p_bre = B_re[pidx], p_bim = B_im[pidx], p_cre = C_re[pidx], p_cim = C_im[pidx];
    u16 o_bre = B_re[pidx ^ 64], o_bim = B_im[pidx ^ 64], o_cre = C_re[pidx ^ 64], o_cim = C_im[pidx ^ 64];
    u32 p_ldt = *(const u32*)(log_dt + d0);
    u32 p_dsk = *(const u32*)(Dskip + d0);
    u32x4 traw[2];
    u32 tdst = 0;
    {
        int pr = b * 7 + (wave < 7 ? wave : 0);
        const u16* src; int N;
        if (pr < 896) { int ntp = pr >> 5, ks = pr & 31; src = w3 + (size_t)(16 * ks) * F + ntp * 64; N = F;
                        tdst = WS_WT3 + ((ntp * 2) * 32 + ks) * 1024; }
        else { int p2 = pr - 896; int ntp = p2 / 112, ks = p2 - ntp * 112; src = w4 + (size_t)(16 * ks) * D + ntp * 64; N = D;
               tdst = WS_WT4 + ((ntp * 2) * 112 + ks) * 1024; }
#pragma unroll
        for (int i = 0; i < 2; ++i) traw[i] = *(const u32x4*)(src + (size_t)(8 * i + (lane >> 3)) * N + (lane & 7) * 8);
    }
    u32x2 tci = *(const u32x2*)(g_tabs.ci + ((wave & 3) < 3 ? (wave & 3) : 0) * 256 + lane * 4);
    u32x4 tex0 = *(const u32x4*)(g_tabs.ex + (tid & 255) * 12);
    u32x2 tex1 = *(const u32x2*)(g_tabs.ex + (tid & 255) * 12 + 8);

    FENCE();
    __builtin_amdgcn_s_barrier();
    u32 xv[4];
    const u16* xcol = x + (size_t)(tid * 4) * D + d0;
#define GATHER(k) do { FENCE(); xv[k] = *(const u32*)(xcol + (k) * D); FENCE(); } while (0)
    GATHER(0);
    const int trq = (lane & 15) >> 2, trp = lane & 3, trh = lane >> 5, trg = (lane >> 4) & 1;
    const int tr_off = (4 * trh + trq) * 128 + (((2 * trg + (trp >> 1)) ^ ((trq >> 1) << 2)) << 4) + 8 * (trp & 1);
    const int dma_sw = ((lane & 7) ^ (((lane >> 4) & 1) << 2)) * 8;
    {
        f32x2 f[4];
#pragma unroll
        for (int i = 0; i < 4; ++i) f[i] = unpk(xr[i]);
        f32x2 s2 = (f[0] + f[1]) + (f[2] + f[3]);
        float mean = wave_sum(s2.x + s2.y) * (1.0f / D);
        f32x2 q2 = {0.f, 0.f};
#pragma unroll
        for (int i = 0; i < 4; ++i) { f32x2 dd = f[i] - mean; q2 += dd * dd; }
        float var = wave_sum(q2.x + q2.y) * (1.0f / D);
        float m, sd;
        ln_consts(mean, var, m, sd);
        const u32 val = (__float_as_uint(m) >> 16) | (__float_as_uint(sd) & 0xffff0000u);
        if (lane == 0)
            __hip_atomic_store((GAS u64*)(ws + WS_XU) + (b * 8 + wave), ((u64)ep << 32) | val, __ATOMIC_RELAXED, __HIP_MEMORY_SCOPE_AGENT);
        GATHER(1);
        LAS char* scr = lds + A_SCR + wave * 2048;
#pragma unroll
        for (int i = 0; i < 2; ++i)
            *(LAS u32x4*)(scr + i * 1024 + (lane >> 3) * 128 + (((lane & 7) ^ (((lane >> 4) & 1) << 2)) << 4)) = traw[i];
        u32x4 f0 = tr_frag(scr + tr_off), f1 = tr_frag(scr + (tr_off ^ 64));
        if (wave < 7) {
            const u32 nstride = tdst < WS_WT4 ? 32 * 1024 : 112 * 1024;
            __builtin_amdgcn_raw_buffer_store_b128(f0, rs, tdst + lane * 16, 0, 16);
            __builtin_amdgcn_raw_buffer_store_b128(f1, rs, tdst + nstride + lane * 16, 0, 16);
        }
        FENCE();
    }
    TS(1);
    float inv_scale[2];
    {
        float dts[2] = {rbf(__expf(bf_lo(p_ldt))), rbf(__expf(bf_hi(p_ldt)))};
        float dt = kch ? dts[1] : dts[0];
        float zr = dt * bf1(p_are), zi = dt * bf1(p_aim);
        float tr = zr * 1.4426950408889634f;
        const float inv2pi_h = 0.15915494309189535f;
        const float inv2pi_l = (float)(0.15915494309189533576888 - (double)inv2pi_h);
        float wh = zi * inv2pi_h;
        float wl = __builtin_fmaf(zi, inv2pi_h, -wh) + zi * inv2pi_l;
        cplx CB = cmul(cplx{bf1(p_cre), bf1(p_cim)}, cplx{dt * bf1(p_bre), dt * bf1(p_bim)});
        cplx CO = cmul(cplx{bf1(o_cre), bf1(o_cim)}, cplx{dt * bf1(o_bre), dt * bf1(o_bim)});
        u32 mx = wave_max_u(max(max(__float_as_uint(CB.x) & 0x7fffffffu, __float_as_uint(CB.y) & 0x7fffffffu),
                                max(__float_as_uint(CO.x) & 0x7fffffffu, __float_as_uint(CO.y) & 0x7fffffffu)));
        int e = (int)(mx >> 23);
        e = e < 8 ? 8 : (e > 246 ? 246 : e);
        float sc = __uint_as_float((u32)(254 + 6 - e) << 23);
        CB.x *= sc; CB.y *= sc;
        if (lane == 0 && knh == 0 && kpart == 0) {
            *(LAS float*)(lds + A_KC + (kch * KC_LEN + 652) * 2) = __uint_as_float((u32)(e - 6) << 23);
            *(LAS u32*)(lds + A_KC + (kch * KC_LEN + KC_ZERO) * 2) = 0u;
        }
        GATHER(2);
        LAS char* Xb = lds + A_X + kch * A_XCH + (knh * 64 + lane) * 4;
        LAS char* Yb = lds + A_Y + kch * A_YCH + (knh * 64 + lane) * 4;
#define PUTX(row, v) *(LAS u32*)(Xb + (row) * TRS) = pk_f16((v).x, (v).y)
#define PUTY(row, v) *(LAS u32*)(Yb + (row) * TRS) = pk_f16((v).x, (v).y)
        if (kpart == 0) {
            cplx P1 = cexp_pow2(tr, wh, wl, 1.f), Q8 = cexp_pow2(tr, -wh, -wl, 8.f), P128 = cexp_pow2(tr, wh, wl, 128.f);
            cplx P2 = csq(P1), Q16 = csq(Q8), P4 = csq(P2), Q32 = csq(Q16), Q64 = csq(Q32);
            cplx Y[8];
            Y[0] = cplx{1.f, 0.f}; Y[1] = Q8; Y[2] = Q16; Y[3] = cmul(Q8, Q16); Y[4] = Q32;
            Y[5] = cmul(Q32, Q8); Y[6] = cmul(Q32, Q16); Y[7] = cmul(Q32, Y[3]);
#pragma unroll
            for (int j = 0; j < 8; ++j) PUTY(j, Y[j]);
            PUTY(8, Q64);
#pragma unroll
            for (int j = 1; j < 8; ++j) { cplx v = cmul(Q64, Y[j]); PUTY(8 + j, v); }
            cplx Xa[8];
            Xa[0] = CB; Xa[1] = cmul(CB, P1); Xa[2] = cmul(CB, P2); Xa[3] = cmul(Xa[2], P1);
#pragma unroll
            for (int j = 0; j < 4; ++j) Xa[4 + j] = cmul(Xa[j], P4);
#pragma unroll
            for (int j = 0; j < 8; ++j) { PUTX(j, Xa[j]); cplx v = cmul(Xa[j], P128); PUTX(8 + j, v); }
        } else {
            cplx P2 = cexp_pow2(tr, wh, wl, 2.f), P128 = cexp_pow2(tr, wh, wl, 128.f), P1024 = cexp_pow2(tr, wh, wl, 1024.f);
            cplx P4 = csq(P2), P256 = csq(P128), P2048 = csq(P1024), P512 = csq(P256);
            cplx X0 = CB, X2 = cmul(CB, P2), X4 = cmul(CB, P4), X6 = cmul(X2, P4);
            cplx X128 = cmul(CB, P128), X132 = cmul(X4, P128);
            cplx c0[4] = {cmul(X0, P256), cmul(X2, P256), cmul(X4, P256), cmul(X6, P256)};
            cplx c1[4];
#pragma unroll
            for (int j = 0; j < 4; ++j) c1[j] = cmul(c0[j], P128);
#pragma unroll
            for (int j = 0; j < 4; ++j) { PUTX(16 + j, c0[j]); PUTX(20 + j, c1[j]); }
            cplx dd[8] = {cmul(X0, P512), cmul(X4, P512), cmul(X128, P512), cmul(X132, P512),
                          cmul(c0[0], P512), cmul(c0[2], P512), cmul(c1[0], P512), cmul(c1[2], P512)};
#pragma unroll
            for (int j = 0; j < 8; ++j) PUTX(24 + j, dd[j]);
            cplx ee[8] = {X0, X128, c0[0], c1[0], dd[0], dd[2], dd[4], dd[6]};
#pragma unroll
            for (int j = 0; j < 8; ++j) { cplx v = cmul(ee[j], P1024); PUTX(32 + j, v); }
            cplx v = cmul(CB, P2048);
            PUTX(40, v);
        }
    }

    LBAR();
    GATHER(3);
    asm volatile("" :: "v"(cw0));
    TS(2);
    if ((wave & 3) < 3) {
        const int mt = wave & 3, r = lane & 15, g = lane >> 4;
        const LAS char* Xb = lds + A_X + kch * A_XCH + (mt * 16 + r) * TRS + g * 16;
        const LAS char* Yb = lds + A_Y + kch * A_YCH + r * TRS + g * 16;
        f32x4 acc0 = {0, 0, 0, 0}, acc1 = {0, 0, 0, 0};
#pragma unroll
        for (int ks = 0; ks < 8; ks += 2) {
            acc0 = mfma16h(*(const LAS u32x4*)(Xb + ks * 64), *(const LAS u32x4*)(Yb + ks * 64), acc0);
            acc1 = mfma16h(*(const LAS u32x4*)(Xb + ks * 64 + 64), *(const LAS u32x4*)(Yb + ks * 64 + 64), acc1);
        }
        acc0 += acc1;
        u32 k01 = pk_f16(acc0[0], acc0[1]), k23 = pk_f16(acc0[2], acc0[3]);
        LAS char* Kc = lds + A_KC + kch * KC_LEN * 2;
        *(LAS u16*)(Kc + (tci[0] & 0xffff)) = (u16)k01;
        *(LAS u16*)(Kc + (tci[0] >> 16)) = (u16)(k01 >> 16);
        *(LAS u16*)(Kc + (tci[1] & 0xffff)) = (u16)k23;
        *(LAS u16*)(Kc + (tci[1] >> 16)) = (u16)(k23 >> 16);
    }
    u32x4 g0 = __builtin_amdgcn_raw_buffer_load_b128(rs, WS_XU + tid * 32, 0, 16);
    u32x4 g1 = __builtin_amdgcn_raw_buffer_load_b128(rs, WS_XU + tid * 32 + 16, 0, 16);
    FENCE();
    LBAR();
    TS(3);
    {
        const int ch = tid >> 8, c = tid & 255;
        const LAS char* Kc = lds + A_KC + ch * KC_LEN * 2;
        u32 pe[6];
        u32 tw[6] = {tex0[0], tex0[1], tex0[2], tex0[3], tex1[0], tex1[1]};
#pragma unroll
        for (int i = 0; i < 6; ++i) {
            u32 lo = *(const LAS u16*)(Kc + (tw[i] & 0xffff));
            u32 hi = *(const LAS u16*)(Kc + (tw[i] >> 16));
            pe[i] = lo | (hi << 16);
        }
        u32 po[5];
#pragma unroll
        for (int i = 0; i < 5; ++i) po[i] = __builtin_amdgcn_alignbit(pe[i + 1], pe[i], 16);
        inv_scale[0] = *(const LAS float*)(lds + A_KC + 652 * 2);
        inv_scale[1] = *(const LAS float*)(lds + A_KC + (KC_LEN + 652) * 2);
        LAS char* Rb = lds + A_R + ch * 4 * RCOPY + c * 16;
        *(LAS u32x4*)(Rb + 0 * RCOPY) = u32x4{pe[0], pe[1], pe[2], pe[3]};
        *(LAS u32x4*)(Rb + 1 * RCOPY) = u32x4{po[0], po[1], po[2], po[3]};
        *(LAS u32x4*)(Rb + 2 * RCOPY) = u32x4{pe[1], pe[2], pe[3], pe[4]};
        *(LAS u32x4*)(Rb + 3 * RCOPY) = u32x4{po[1], po[2], po[3], po[4]};
        if (tid < 16) *(LAS u32x4*)(lds + A_R + (tid >> 1) * RCOPY + 4096 + (tid & 1) * 16) = u32x4{0, 0, 0, 0};
        if (tid >= 64 && tid < 124) {
            *(LAS u32x2*)(lds + A_U + (tid - 64) * 8) = u32x2{0, 0};
            *(LAS u32x2*)(lds + A_U + ULEN * 2 + (tid - 64) * 8) = u32x2{0, 0};
        }
    }

    _Pragma("unroll 1") for (int spin = 0; spin < SPIN_MAX; ++spin) {
        const bool ok = g0[1] == ep && g0[3] == ep && g1[1] == ep && g1[3] == ep;
        if (__all(ok)) break;
        __builtin_amdgcn_s_sleep(2);
        FENCE();
        g0 = __builtin_amdgcn_raw_buffer_load_b128(rs, WS_XU + tid * 32, 0, 16);
        g1 = __builtin_amdgcn_raw_buffer_load_b128(rs, WS_XU + tid * 32 + 16, 0, 16);
    }
    u32 udp[4];
    {
        const u32 st[4] = {g0[0], g0[2], g1[0], g1[2]};
        const f32x2 dsk = unpk(p_dsk);
        f32x2 u[4];
#pragma unroll
        for (int k = 0; k < 4; ++k) {
            const float m = bf_lo(st[k]), rsd = __builtin_amdgcn_rcpf(bf_hi(st[k]));
            const f32x2 dlt = unpk(pk_bf16v(unpk(xv[k]) - m));
            u[k] = unpk(pk_bf16v(dlt * rsd));
            udp[k] = pk_bf16v(u[k] * dsk);
        }
        *(LAS u32x2*)(lds + A_U + (UPAD + tid * 4) * 2) = u32x2{pk_f16(u[0].x, u[1].x), pk_f16(u[2].x, u[3].x)};
        *(LAS u32x2*)(lds + A_U + ULEN * 2 + (UPAD + tid * 4) * 2) = u32x2{pk_f16(u[0].y, u[1].y), pk_f16(u[2].y, u[3].y)};
    }
    FENCE();
    u32 ww0 = 0, ww1 = 0;
    if (wave == 2 || wave == 3) {
        const u32 wo = (bq * 128 + lane) * 128;
        if (wave == 2) {
            asm volatile("buffer_load_dword %0, %1, %2, 0 offen" : "=v"(ww0) : "v"(wo), "s"(srd_w1) : "memory");
            asm volatile("buffer_load_dword %0, %1, %2, 0 offen" : "=v"(ww1) : "v"(wo + 8192), "s"(srd_w1) : "memory");
        } else {
            asm volatile("buffer_load_dword %0, %1, %2, 0 offen" : "=v"(ww0) : "v"(wo), "s"(srd_w2) : "memory");
            asm volatile("buffer_load_dword %0, %1, %2, 0 offen" : "=v"(ww1) : "v"(wo + 8192), "s"(srd_w2) : "memory");
        }
    }
    const u32 w3off = wave < 7 ? WS_WT3 + (ct * 7 + wave) * 32768 + lane * 16 : 0xF0000000u;
    u32x4 w3f[32];
    float c3a = 0.f, c3b = 0.f;
#define CS3(lo, hi) do { _Pragma("unroll") for (int i_ = (lo); i_ < (hi); ++i_) { \
        c3a = dot2bf(w3f[i_][0], 0x3f803f80u, c3a); c3b = dot2bf(w3f[i_][1], 0x3f803f80u, c3b); \
        c3a = dot2bf(w3f[i_][2], 0x3f803f80u, c3a); c3b = dot2bf(w3f[i_][3], 0x3f803f80u, c3b); } } while (0)
#define W3L1(i) w3f[i] = __builtin_amdgcn_raw_buffer_load_b128(rs, w3off + (i) * 1024, 0, 16)
#define W3LOAD(lo, hi) do { FENCE(); _Pragma("unroll") for (int i_ = (lo); i_ < (hi); ++i_) \
        w3f[i_] = __builtin_amdgcn_raw_buffer_load_b128(rs, w3off + i_ * 1024, 0, 16); FENCE(); } while (0)
    const u32 img_voff = (lane >> 3) * (D * 2) + ct * 128 + dma_sw * 2;
    LBAR();
    TS(4);
    {
        const int ch = wave >> 2, rq = wave & 3;
        const int r = lane & 15, g = lane >> 4;
        const int o0 = 15 - r + 8 * g;
        const LAS char* Ab = lds + A_R + ch * 4 * RCOPY + (o0 & 3) * RCOPY + (o0 & ~3) * 2 + (6 - rq * 2) * 64;
        const LAS char* Bb = lds + A_U + ch * ULEN * 2 + (16 * r + 8 * g) * 2 + (6 - rq * 2) * 64;
        f32x4 acc[8];
#pragma unroll
        for (int i = 0; i < 8; ++i) acc[i] = f32x4{0, 0, 0, 0};
        u32x4 A0[8], B0[8], A1[8], B1[8];
#define LD1(AF, BF, a, rr) do { \
            u32x2 lo_ = *(const volatile LAS u32x2*)(Ab + (1 - (rr)) * 64 + (7 - (a)) * 512); \
            u32x2 hi_ = *(const volatile LAS u32x2*)(Ab + (1 - (rr)) * 64 + (7 - (a)) * 512 + 8); \
            AF[a] = u32x4{lo_[0], lo_[1], hi_[0], hi_[1]}; \
            BF[a] = *(const LAS u32x4*)(Bb + (1 - (rr)) * 64 + (a) * 512); } while (0)
#define GRP(AF, BF, m) do { _Pragma("unroll") for (int o_ = 0; o_ <= (m); ++o_) if (o_ + (m) <= 7) { \
            acc[o_ + (m)] = mfma16h(AF[m], BF[o_], acc[o_ + (m)]); \
            if (o_ != (m)) acc[o_ + (m)] = mfma16h(AF[o_], BF[m], acc[o_ + (m)]); } } while (0)
        LD1(A0, B0, 0, 0); LD1(A0, B0, 7, 0); LD1(A0, B0, 1, 0); LD1(A0, B0, 6, 0);
        SCHED0();
        GRP(A0, B0, 7); LD1(A0, B0, 2, 0); LD1(A0, B0, 5, 0); SCHED0();
        GRP(A0, B0, 6); LD1(A0, B0, 3, 0); LD1(A0, B0, 4, 0); SCHED0();
        GRP(A0, B0, 5); SCHED0();
        GRP(A0, B0, 4); LD1(A1, B1, 0, 1); SCHED0();
        GRP(A0, B0, 3); LD1(A1, B1, 1, 1); SCHED0();
        GRP(A0, B0, 2); LD1(A1, B1, 2, 1); SCHED0();
        GRP(A0, B0, 1); LD1(A1, B1, 3, 1); SCHED0();
        GRP(A0, B0, 0); GRP(A1, B1, 0); LD1(A1, B1, 4, 1); SCHED0();
        GRP(A1, B1, 1); LD1(A1, B1, 5, 1); SCHED0();
        GRP(A1, B1, 2); LD1(A1, B1, 6, 1); SCHED0();
        GRP(A1, B1, 3); LD1(A1, B1, 7, 1); SCHED0();
        GRP(A1, B1, 4); SCHED0();
        GRP(A1, B1, 5); SCHED0();
        GRP(A1, B1, 6); SCHED0();
        GRP(A1, B1, 7);
        LAS char* Yp = lds + A_YP + (rq * 2 + ch) * 8192 + g * 256 + ((r ^ (4 * g) ^ ch) << 4);
#pragma unroll
        for (int c = 0; c < 8; ++c) *(LAS f32x4*)(Yp + 1024 * c) = acc[c];
    }
    LBAR();
    TS(5);
    {
        const int sl = (lane >> 2) ^ (4 * (lane & 3));
        const LAS char* Y0 = lds + A_YP + wave * 1024 + (lane & 3) * 256 + (sl << 4);
        const LAS char* Y1 = lds + A_YP + 8192 + wave * 1024 + (lane & 3) * 256 + ((sl ^ 1) << 4);
        f32x4 y0 = ((*(const LAS f32x4*)(Y0) + *(const LAS f32x4*)(Y0 + 16384)) +
                    (*(const LAS f32x4*)(Y0 + 32768) + *(const LAS f32x4*)(Y0 + 49152))) * inv_scale[0];
        f32x4 y1 = ((*(const LAS f32x4*)(Y1) + *(const LAS f32x4*)(Y1 + 16384)) +
                    (*(const LAS f32x4*)(Y1 + 32768) + *(const LAS f32x4*)(Y1 + 49152))) * inv_scale[1];
        u32 hp[4];
#pragma unroll
        for (int k = 0; k < 4; ++k) {
            f32x2 yy = unpk(pk_bf16(y0[k], y1[k]));
            f32x2 a = unpk(pk_bf16v(yy + unpk(udp[k])));
            hp[k] = pk_bf16v(unpk(xv[k]) + a);
        }
        u32 c00 = __builtin_amdgcn_perm(hp[1], hp[0], 0x05040100), c01 = __builtin_amdgcn_perm(hp[3], hp[2], 0x05040100);
        u32 c10 = __builtin_amdgcn_perm(hp[1], hp[0], 0x07060302), c11 = __builtin_amdgcn_perm(hp[3], hp[2], 0x07060302);
        const bool odd = lane & 1;
        u32 s0 = odd ? c00 : c10, s1 = odd ? c01 : c11;
        u32 r0 = __builtin_amdgcn_update_dpp(0, s0, 0xB1, 0xf, 0xf, true);
        u32 r1 = __builtin_amdgcn_update_dpp(0, s1, 0xB1, 0xf, 0xf, true);
        u32x4 o = odd ? u32x4{r0, r1, c10, c11} : u32x4{c00, c01, r0, r1};
        __builtin_amdgcn_raw_buffer_store_b128(o, rs, WS_HC + (tid >> 4) * 65536 + (d0 + (odd ? 1 : 0)) * 128 + ((tid >> 1) & 7) * 16, 0, 16);
    }
    LBAR();
#pragma unroll
    for (int i = 0; i < 8; ++i) dma_in(srd_w1, img_voff, (wave * 8 + i) * 8 * D * 2, lbase + G_ACT + (wave * 8 + i) * 1024);
    WAIT_V(8);
    LBAR();
    if (tid < 8) __hip_atomic_store((GAS u32*)(ws + WS_F1) + tid * 256 + b, ep, __ATOMIC_RELAXED, __HIP_MEMORY_SCOPE_AGENT);
    asm volatile("" :: "v"(ww0), "v"(ww1));
    TS(6);
#pragma unroll
    for (int i = 0; i < 8; ++i) dma_in(srd_w2, img_voff, (wave * 8 + i) * 8 * D * 2, lbase + A_W + (wave * 8 + i) * 1024);
    WAIT_V(8);
    LBAR();

    const int tq = wave & 1, nq = (wave >> 1) & 1, kh = wave >> 2;
    const int r32 = lane & 31, h32 = lane >> 5;
    const u32 ones = 0x3f803f80u;
    u32x4 wf[32];
    {
        const LAS char* WbE = lds + G_ACT + kh * 8192 + (tr_off ^ (nq << 6)) + nq * 2048;
        const LAS char* WbO = WbE - nq * 4096;
#pragma unroll
        for (int i = 0; i < 16; ++i) wf[i] = tr_frag(((i & 1) ? WbO : WbE) + ((i >> 2) * 8 + (i & 3)) * 2048);
    }
    if (wave == 7) {
        _Pragma("unroll 1") for (int spin = 0; spin < SPIN_MAX; ++spin) {
            FENCE();
            u32x4 f = __builtin_amdgcn_raw_buffer_load_b128(rs, WS_F1 + (b & 7) * 1024 + lane * 16, 0, 16);
            if (__all(f[0] == ep && f[1] == ep && f[2] == ep && f[3] == ep)) break;
            __builtin_amdgcn_s_sleep(1);
        }
        if (b == 0 && lane == 0) *(GAS u32*)(ws + WS_CTL) = ep;
    }
    LBAR();
    TS(7);
    {
        const u32 voff = (lane >> 3) * 128 + dma_sw * 2;
#pragma unroll
        for (int c = 0; c < 4; ++c)
            dma_ws2(srd_ws, voff, WS_HC + tt * 65536 + (8 * c + wave) * 2048, lbase + G_ACT + (8 * c + wave) * 2048);
    }
    {
        float ca = 0.f, cb_ = 0.f;
#pragma unroll
        for (int i = 0; i < 16; ++i) {
            ca = dot2bf(wf[i][0], ones, ca); cb_ = dot2bf(wf[i][1], ones, cb_);
            ca = dot2bf(wf[i][2], ones, ca); cb_ = dot2bf(wf[i][3], ones, cb_);
        }
        const float csum = add_xor32(ca + cb_);
        if (h32 == 0 && tq == 0) *(LAS float*)(lds + G_CS + ((nq * 2 + kh) * 32 + r32) * 4) = csum;
    }
    u32x4 hres, x1res;
    {
        f32x16 acc = {};
        u32x4 own[2];
        float s1a = 0.f, s1b = 0.f, q1a = 0.f, q1b = 0.f;
        const LAS char* Ab = lds + G_ACT + (tr_off ^ (tq << 6));
        const LAS char* AbE = Ab + kh * 8192 + nq * 2048;
        const LAS char* AbO = AbE - nq * 4096;
#pragma unroll
        for (int c = 0; c < 4; ++c) {
            if (c == 0) WAIT_V(6); else if (c == 1) WAIT_V(4); else if (c == 2) WAIT_V(2); else WAIT_V(0);
            LBAR();
#pragma unroll
            for (int kk = 0; kk < 4; ++kk) {
                u32x4 bb = tr_frag(((kk & 1) ? AbO : AbE) + (8 * c + kk) * 2048);
                acc = mfma32(wf[4 * c + kk], bb, acc);
                if ((kk & 1) == 0) own[kk >> 1] = bb;
            }
#pragma unroll
            for (int i = 0; i < 2; ++i) {
                const u32x4 v = own[i];
                s1a = dot2bf(v[0], ones, s1a); s1b = dot2bf(v[1], ones, s1b);
                q1a = dot2bf(v[0], v[0], q1a); q1b = dot2bf(v[1], v[1], q1b);
                s1a = dot2bf(v[2], ones, s1a); s1b = dot2bf(v[3], ones, s1b);
                q1a = dot2bf(v[2], v[2], q1a); q1b = dot2bf(v[3], v[3], q1b);
            }
        }
        hres = tr_frag(Ab + (4 * ct + 2 * nq + kh) * 2048);
        W3LOAD(0, 8);
        TS(8);
        {
            LAS char* p = lds + G_RED + wave * 2048 + lane * 16;
            if (kh == 0) { *(LAS f32x4*)(p) = f32x4{acc[8], acc[9], acc[10], acc[11]}; *(LAS f32x4*)(p + 1024) = f32x4{acc[12], acc[13], acc[14], acc[15]}; }
            else { *(LAS f32x4*)(p) = f32x4{acc[0], acc[1], acc[2], acc[3]}; *(LAS f32x4*)(p + 1024) = f32x4{acc[4], acc[5], acc[6], acc[7]}; }
        }
        float s1 = add_xor32(s1a + s1b), q1 = add_xor32(q1a + q1b);
        LAS char* ps = lds + G_PS + (tq * 32 + r32) * 32;
        if (h32 == 0) *(LAS f32x2*)(ps + (nq * 2 + kh) * 8) = f32x2{s1, q1};
        LBAR();
        TS(9);
    float csv[8];
    {
        const LAS char* p = lds + G_CS + nq * 256 + (16 * kh + 4 * h32) * 4;
        f32x4 a0 = *(const LAS f32x4*)(p), a1 = *(const LAS f32x4*)(p + 32);
        f32x4 b0 = *(const LAS f32x4*)(p + 128), b1 = *(const LAS f32x4*)(p + 160);
#pragma unroll
        for (int e = 0; e < 4; ++e) { csv[e] = a0[e] + b0[e]; csv[4 + e] = a1[e] + b1[e]; }
    }
        float fin[8];
        {
            const LAS char* p = lds + G_RED + (wave ^ 4) * 2048 + lane * 16;
            f32x4 a0 = *(const LAS f32x4*)(p), a1 = *(const LAS f32x4*)(p + 1024);
#pragma unroll
            for (int e = 0; e < 4; ++e) {
                fin[e] = a0[e] + (kh ? acc[8 + e] : acc[e]);
                fin[4 + e] = a1[e] + (kh ? acc[12 + e] : acc[4 + e]);
            }
        }
        float m1, sd1, m2, sd2;
        f32x4 p0 = *(const LAS f32x4*)(ps), p1 = *(const LAS f32x4*)(ps + 16);
        const float mean = ((p0[0] + p0[2]) + (p1[0] + p1[2])) * (1.0f / 512);
        const float msq = ((p0[1] + p0[3]) + (p1[1] + p1[3])) * (1.0f / 512);
        const float var = fmaxf(msq - mean * mean, 0.f);
        ln_consts(mean, var, m1, sd1);
        const float r1 = __builtin_amdgcn_rcpf(sd1), nb1 = -m1 * r1;
        ln_consts((mean - m1) * r1, var * r1 * r1, m2, sd2);
        const float alpha = r1 * __builtin_amdgcn_rcpf(sd2);
        const float mu = m1 + m2 * sd1;
        const float ga = GC1 * alpha, gb = GC3 * alpha * alpha * alpha;
        u32x4 o;
#pragma unroll
        for (int e = 0; e < 4; ++e) {
            float d0_ = __builtin_fmaf(-mu, csv[2 * e], fin[2 * e]), d1_ = __builtin_fmaf(-mu, csv[2 * e + 1], fin[2 * e + 1]);
            o[e] = pk_bf16(gelu_s(d0_, alpha, ga, gb), gelu_s(d1_, alpha, ga, gb));
        }
        __builtin_amdgcn_raw_buffer_store_b128(o, rs, WS_HG + (((tt * 2 + tq) * 32 + 4 * ct + 2 * nq + kh) * 64 + lane) * 16, 0, 16);
#pragma unroll
        for (int e = 0; e < 4; ++e)
            x1res[e] = pk_bf16(__builtin_fmaf(bf_lo(hres[e]), r1, nb1), __builtin_fmaf(bf_hi(hres[e]), r1, nb1));
    }
    WAIT_V(0);
    LBAR();
    if (tid == 0) __hip_atomic_store((GAS u32*)(ws + WS_F2) + (tt * 8 + ct) * 32, ep, __ATOMIC_RELAXED, __HIP_MEMORY_SCOPE_AGENT);
    W3LOAD(8, 24);
    TS(10);

    {
        const LAS char* Wb = lds + A_W + kh * 8192 + (tr_off ^ (nq << 6));
#pragma unroll
        for (int i = 0; i < 16; ++i) wf[i] = tr_frag(Wb + ((i >> 2) * 8 + (i & 3)) * 2048);
    }
    if (wave < 7) CS3(0, 8);
    if (wave == 7) {
        const int tl = tt - 1 + (lane >> 3);
        const bool act = lane < 24 && tl >= 0 && tl < 32;
        const GAS u32* fp = (const GAS u32*)(ws + WS_F2) + ((act ? tl : tt) * 8 + (lane & 7)) * 32;
        _Pragma("unroll 1") for (int spin = 0; spin < SPIN_MAX; ++spin) {
            u32 f = __hip_atomic_load(fp, __ATOMIC_RELAXED, __HIP_MEMORY_SCOPE_AGENT);
            if (__all(f == ep)) break;
            __builtin_amdgcn_s_sleep(1);
        }
    }
    LBAR();
    TS(11);
#pragma unroll
    for (int c = 0; c < 4; ++c) {
        const int ks = 8 * c + wave;
        const int sh = c == 0 ? -1 : (c == 1 ? 1 : (c == 2 ? -2 : 2));
#pragma unroll
        for (int i = 0; i < 2; ++i) {
            const int t = tt * 64 + i * 32 + r32 + sh;
            const bool ok = (unsigned)t < (unsigned)L;
            const char* src = ok ? ws + WS_HG + ((((t >> 5) * 32 + ks) * 64) + h32 * 32 + (t & 31)) * 16 : (const char*)g_zero16;
            dma_g(src, lbase + G_ACT + (i * 32 + ks) * 1024);
        }
    }
    {
        f32x16 acc = {};
#pragma unroll
        for (int c = 0; c < 4; ++c) {
            if (c == 0) WAIT_V(6); else if (c == 1) WAIT_V(4); else if (c == 2) WAIT_V(2); else WAIT_V(0);
            LBAR();
#pragma unroll
            for (int kk = 0; kk < 4; ++kk) {
                const int ks = 8 * c + 4 * kh + kk;
                u32x4 bb = *(const LAS u32x4*)(lds + G_ACT + (tq * 32 + ks) * 1024 + lane * 16);
                acc = mfma32(wf[4 * c + kk], bb, acc);
            }
        }
        W3LOAD(24, 32);
        {
            LAS char* p = lds + G_RED + wave * 2048 + lane * 16;
            if (kh == 0) { *(LAS f32x4*)(p) = f32x4{acc[8], acc[9], acc[10], acc[11]}; *(LAS f32x4*)(p + 1024) = f32x4{acc[12], acc[13], acc[14], acc[15]}; }
            else { *(LAS f32x4*)(p) = f32x4{acc[0], acc[1], acc[2], acc[3]}; *(LAS f32x4*)(p + 1024) = f32x4{acc[4], acc[5], acc[6], acc[7]}; }
        }
        LBAR();
        TS(12);
        const LAS char* p = lds + G_RED + (wave ^ 4) * 2048 + lane * 16;
        f32x4 a0 = *(const LAS f32x4*)(p), a1 = *(const LAS f32x4*)(p + 1024);
        float fin[8];
#pragma unroll
        for (int e = 0; e < 4; ++e) {
            fin[e] = a0[e] + (kh ? acc[8 + e] : acc[e]);
            fin[4 + e] = a1[e] + (kh ? acc[12 + e] : acc[4 + e]);
        }
#pragma unroll
        for (int e = 0; e < 4; ++e)
            x1res[e] = pk_bf16v(unpk(x1res[e]) + unpk(pk_bf16(fin[2 * e], fin[2 * e + 1])));
        __builtin_amdgcn_raw_buffer_store_b128(x1res, rs, WS_X1 + (((tt * 2 + tq) * 32 + 4 * ct + 2 * nq + kh) * 64 + lane) * 16, 0, 16);
        {
            float sa = 0.f, qa = 0.f, sb = 0.f, qb = 0.f;
#pragma unroll
            for (int e = 0; e < 4; ++e) {
                const float lo = bf_lo(x1res[e]), hi = bf_hi(x1res[e]);
                sa += lo; sb += hi; qa = __builtin_fmaf(lo, lo, qa); qb = __builtin_fmaf(hi, hi, qb);
            }
            const float st = add_xor32(sa + sb), qt = add_xor32(qa + qb);
            if (h32 == 0)
                __builtin_amdgcn_raw_buffer_store_b64(u32x2{__float_as_uint(st), __float_as_uint(qt)}, rs,
                                                      WS_PST + ((tt * 2 + tq) * 32 + ct * 4 + nq * 2 + kh) * 256 + r32 * 8, 0, 16);
        }
        *(LAS u32x4*)(lds + G_SPARE + wave * 2048 + lane * 16) = x1res;
        *(LAS u32x4*)(lds + G_SPARE + wave * 2048 + 1024 + lane * 16) = hres;
    }
    WAIT_V(0);
    LBAR();
    if (tid == 0) __hip_atomic_store((GAS u32*)(ws + WS_F3) + (tt * 8 + ct) * 32, ep, __ATOMIC_RELAXED, __HIP_MEMORY_SCOPE_AGENT);
    TS(13);
    const int kq = 2 * tq + kh;
    const u32 w4base = WS_WT4 + ((ct * 2 + nq) * 112 + kq * 28) * 1024;
    const u32 w4st = wave < 2 ? 65536 + wave * 8192 : (wave < 4 ? 81920 + wave * 8192 : 98304 + wave * 8192);
    dma_ws4(srd_ws, lane * 16, w4base + 20 * 1024, lbase + w4st);
    dma_ws4(srd_ws, lane * 16, w4base + 24 * 1024, lbase + w4st + 4096);

    const u32 w4off = w4base + lane * 16;
    u32x4 w4f[28];
    const GAS u32* f4p = (const GAS u32*)(ws + WS_F4) + (tt * 8 + (lane & 7)) * 32;
#define ACT3_DMA() do { \
        dma_ws4(srd_ws, lane * 16, WS_X1 + tt * 65536 + wave * 4096, lbase + G_ACT + wave * 4096); \
        dma_ws4(srd_ws, lane * 16, WS_X1 + tt * 65536 + 32768 + wave * 4096, lbase + G_ACT + 32768 + wave * 4096); } while (0)
#define POLL(ptr) do { _Pragma("unroll 1") for (int spin_ = 0; spin_ < SPIN_MAX; ++spin_) { \
        u32 f_ = __hip_atomic_load((ptr), __ATOMIC_RELAXED, __HIP_MEMORY_SCOPE_AGENT); \
        if (__all(f_ == ep)) break; \
        __builtin_amdgcn_s_sleep(1); } } while (0)
    if (wave == 7) {
        if (lane == 0) *(LAS u32*)(lds + G_CS + 2048) = 0u;
        POLL((const GAS u32*)(ws + WS_F3) + (tt * 8 + (lane & 7)) * 32);
        LBAR();
        ACT3_DMA();
        u32x2 pp[2][16];
#pragma unroll
        for (int i = 0; i < 2; ++i)
#pragma unroll
            for (int j = 0; j < 16; ++j)
                pp[i][j] = __builtin_amdgcn_raw_buffer_load_b64(rs, WS_PST + ((tt * 2 + i) * 32 + h32 * 16 + j) * 256 + r32 * 8, 0, 16);
        FENCE();
        WAIT_V(36);
        LBAR();
#pragma unroll
        for (int i = 0; i < 2; ++i) {
            float sa = 0.f, qa = 0.f, sb = 0.f, qb = 0.f;
#pragma unroll
            for (int j = 0; j < 16; j += 2) {
                sa += __uint_as_float(pp[i][j][0]); qa += __uint_as_float(pp[i][j][1]);
                sb += __uint_as_float(pp[i][j + 1][0]); qb += __uint_as_float(pp[i][j + 1][1]);
            }
            float mean = add_xor32(sa + sb) * (1.0f / 512), msq = add_xor32(qa + qb) * (1.0f / 512);
            float m, sd;
            ln_consts(mean, fmaxf(msq - mean * mean, 0.f), m, sd);
            if (h32 == 0) *(LAS f32x2*)(lds + G_PS + (i * 32 + r32) * 8) = f32x2{__builtin_amdgcn_rcpf(sd), m};
        }
        FENCE();
#pragma unroll
        for (int j = 0; j < 10; ++j) { w4f[j] = __builtin_amdgcn_raw_buffer_load_b128(rs, w4off + j * 1024, 0, 16); __builtin_amdgcn_s_sleep(2); }
        FENCE();
        LBAR();
#pragma unroll
        for (int j = 10; j < 20; ++j) { w4f[j] = __builtin_amdgcn_raw_buffer_load_b128(rs, w4off + j * 1024, 0, 16); __builtin_amdgcn_s_sleep(2); }
        FENCE();
#pragma unroll
        for (int j = 0; j < 8; ++j) w4f[20 + j] = *(const LAS u32x4*)(lds + w4st + j * 1024 + lane * 16);
        LBAR();
        POLL(f4p);
#pragma unroll
        for (int i = 0; i < 28; ++i) PIN4(w4f[i]);
        LBAR();
    } else {
        CS3(8, 32);
        {
            const float csum = add_xor32(c3a + c3b);
            if (h32 == 0) *(LAS float*)(lds + G_CS + (wave * 32 + r32) * 4) = csum;
        }
        LBAR();
        TS(14);
        ACT3_DMA();
        const LAS char* Ab = lds + G_ACT + lane * 16;
        const u32 gdst = WS_G + ((tt * 2 * 112 + (ct * 7 + wave) * 2) * 64 + lane) * 16;
        f32x16 acc0 = {}, acc1 = {};
        u32x4 bq[4];
        WAIT_V(4);
        LBAR();
        TS(30);
#pragma unroll
        for (int i = 0; i < 4; ++i) bq[i] = *(const LAS u32x4*)(Ab + i * 1024);
#pragma unroll
        for (int ks = 0; ks < 32; ++ks) {
            acc0 = mfma32(w3f[ks], bq[ks & 3], acc0);
            if (ks + 4 < 32) bq[ks & 3] = *(const LAS u32x4*)(Ab + (ks + 4) * 1024);
            if (ks % 3 == 0) w4f[ks / 3] = __builtin_amdgcn_raw_buffer_load_b128(rs, w4off + (ks / 3) * 1024, 0, 16);
            SCHED0();
        }
        TS(31);
        WAIT_V(11);
        LBAR();
        TS(15);
#pragma unroll
        for (int i = 0; i < 4; ++i) bq[i] = *(const LAS u32x4*)(Ab + (32 + i) * 1024);
        float cs[16];
#pragma unroll
        for (int i = 0; i < 4; ++i) {
            f32x4 c4 = *(const LAS f32x4*)(lds + G_CS + (wave * 32 + 8 * i + 4 * h32) * 4);
            cs[4 * i] = c4[0]; cs[4 * i + 1] = c4[1]; cs[4 * i + 2] = c4[2]; cs[4 * i + 3] = c4[3];
        }
        {
            const f32x2 st = *(const LAS f32x2*)(lds + G_PS + r32 * 8);
            const float ga = GC1 * st.x, gb = GC3 * st.x * st.x * st.x;
            float g[16];
            SCHED0();
#pragma unroll
            for (int k4 = 0; k4 < 32; k4 += 4) {
#pragma unroll
                for (int ks = k4; ks < k4 + 4; ++ks) {
                    acc1 = mfma32(w3f[ks], bq[ks & 3], acc1);
                    if (ks + 4 < 32) bq[ks & 3] = *(const LAS u32x4*)(Ab + (32 + ks + 4) * 1024);
                    if (ks < 16) g[ks] = gelu_s(__builtin_fmaf(-st.y, cs[ks], acc0[ks]), st.x, ga, gb);
                    if (ks >= 20 && ks < 29) w4f[11 + ks - 20] = __builtin_amdgcn_raw_buffer_load_b128(rs, w4off + (11 + ks - 20) * 1024, 0, 16);
                    if (ks >= 24) w4f[20 + ks - 24] = *(const LAS u32x4*)(lds + w4st + (ks - 24) * 1024 + lane * 16);
                }
                if (k4 == 8 || k4 == 16) {
                    const int o = k4 - 8;
                    u32x4 v = {pk_bf16(g[o], g[o + 1]), pk_bf16(g[o + 2], g[o + 3]), pk_bf16(g[o + 4], g[o + 5]), pk_bf16(g[o + 6], g[o + 7])};
                    __builtin_amdgcn_raw_buffer_store_b128(v, rs, gdst + (o >> 3) * 1024, 0, 16);
                }
#pragma unroll
                for (int q = 0; q < 4; ++q) {
                    __builtin_amdgcn_sched_group_barrier(0x008, 1, 0);
                    __builtin_amdgcn_sched_group_barrier(0x100, 1, 0);
                    __builtin_amdgcn_sched_group_barrier(0x002, 10, 0);
                    __builtin_amdgcn_sched_group_barrier(0x010, 1, 0);
                }
                SCHED0();
            }
        }
        WAIT_V(9);
        LBAR();
        if (tid == 0) __hip_atomic_store((GAS u32*)(ws + WS_F4) + (tt * 8 + ct) * 32, ep, __ATOMIC_RELAXED, __HIP_MEMORY_SCOPE_AGENT);
        TS(16);
        {
            const f32x2 st = *(const LAS f32x2*)(lds + G_PS + (32 + r32) * 8);
            const float ga = GC1 * st.x, gb = GC3 * st.x * st.x * st.x;
#pragma unroll
            for (int sI = 0; sI < 2; ++sI) {
                u32x4 v;
#pragma unroll
                for (int e = 0; e < 4; ++e) {
                    const int j = 8 * sI + 2 * e;
                    v[e] = pk_bf16(gelu_s(__builtin_fmaf(-st.y, cs[j], acc1[j]), st.x, ga, gb),
                                   gelu_s(__builtin_fmaf(-st.y, cs[j + 1], acc1[j + 1]), st.x, ga, gb));
                }
                __builtin_amdgcn_raw_buffer_store_b128(v, rs, gdst + 112 * 1024 + sI * 1024, 0, 16);
            }
        }
        TS(17);
        WAIT_V(0);
        if (lane == 0) {
            const u32 old = __hip_atomic_fetch_add((LAS u32*)(lds + G_CS + 2048), 1u, __ATOMIC_RELAXED, __HIP_MEMORY_SCOPE_WORKGROUP);
            if (old == 6) __hip_atomic_store((GAS u32*)(ws + WS_F5) + (tt * 8 + ct) * 32, ep, __ATOMIC_RELAXED, __HIP_MEMORY_SCOPE_AGENT);
        }
#pragma unroll
        for (int i = 0; i < 28; ++i) PIN4(w4f[i]);
        LBAR();
    }
    TS(18);
    {
        const u32 gsrc = WS_G + (tt * 2 * 112 + wave * 14) * 1024;
        const u32 ldst = lbase + wave * 14336;
#define FILL_A(t) do { dma_ws4(srd_ws, lane * 16, gsrc + (t) * 114688, ldst); dma_ws4(srd_ws, lane * 16, gsrc + (t) * 114688 + 4096, ldst + 4096); } while (0)
#define FILL_B(t, dst) do { dma_ws4(srd_ws, lane * 16, gsrc + (t) * 114688 + 8192, (dst)); dma_ws2(srd_ws, lane * 16, gsrc + (t) * 114688 + 12288, (dst) + 4096); } while (0)
        FILL_A(0); FILL_B(0, ldst + 8192);
        x1res = *(const LAS u32x4*)(lds + G_SPARE + wave * 2048 + lane * 16);
        hres = *(const LAS u32x4*)(lds + G_SPARE + wave * 2048 + 1024 + lane * 16);
        const LAS char* Gb = lds + kq * 28672 + lane * 16;
        const LAS char* Gs = lds + G_SPARE + kq * 12288 + lane * 16;
        f32x16 acc0 = {}, acc1 = {};
        u32x4 bq[4];
#define KA(i) ((i) < 8 ? (i) : (i) + 6)
#define KB(i) ((i) < 6 ? (i) + 8 : (i) + 16)
#define KS(i) (i)
#define PART(ACC, BASE, KF, KW, N) do { \
        _Pragma("unroll") for (int i_ = 0; i_ < 4; ++i_) bq[i_] = *(const LAS u32x4*)(BASE + KF(i_) * 1024); \
        _Pragma("unroll") for (int i_ = 0; i_ < N; ++i_) { \
            ACC = mfma32(w4f[KW(i_)], bq[i_ & 3], ACC); \
            if (i_ + 4 < N) bq[i_ & 3] = *(const LAS u32x4*)(BASE + KF(i_ + 4) * 1024); \
            SCHED0(); } } while (0)
        if (wave == 7) POLL(f4p + 8192);
        WAIT_V(6);
        LBAR();
        TS(20);
        {
#pragma unroll
            for (int i_ = 0; i_ < 4; ++i_) bq[i_] = *(const LAS u32x4*)(Gb + KA(i_) * 1024);
#pragma unroll
            for (int i_ = 0; i_ < 16; ++i_) {
                if (i_ % 4 == 1 && i_ < 12) dma_ws2(srd_ws, lane * 16, gsrc + 114688 + 8192 + (i_ / 4) * 2048, lbase + G_SPARE + wave * 6144 + (i_ / 4) * 2048);
                acc0 = mfma32(w4f[KA(i_)], bq[i_ & 3], acc0);
                if (i_ + 4 < 16) bq[i_ & 3] = *(const LAS u32x4*)(Gb + KA(i_ + 4) * 1024);
                SCHED0();
            }
        }
        WAIT_V(6);
        LBAR();
        TS(21);
        {
#pragma unroll
            for (int i_ = 0; i_ < 4; ++i_) bq[i_] = *(const LAS u32x4*)(Gb + KB(i_) * 1024);
#pragma unroll
            for (int i_ = 0; i_ < 12; ++i_) {
                if (i_ < 4) dma_ws2(srd_ws, lane * 16, gsrc + 114688 + i_ * 2048, ldst + i_ * 2048);
                acc0 = mfma32(w4f[KB(i_)], bq[i_ & 3], acc0);
                if (i_ + 4 < 12) bq[i_ & 3] = *(const LAS u32x4*)(Gb + KB(i_ + 4) * 1024);
                SCHED0();
            }
        }
        WAIT_V(8);
        LBAR();
        TS(22);
        PART(acc1, Gs, KS, KB, 12);
        WAIT_V(0);
        LBAR();
        TS(23);
        PART(acc1, Gb, KA, KA, 16);
        TS(24);
#pragma unroll
        for (int p = 0; p < 4; ++p) {
            if (p != kq) {
                const int slot = kq > p ? kq - 1 : kq;
                LAS char* dst = lds + G_SPARE + ((nq * 4 + p) * 3 + slot) * 2048 + lane * 16;
                const int o = 8 * (p & 1);
                if (p < 2) { *(LAS f32x4*)(dst) = f32x4{acc0[o], acc0[o + 1], acc0[o + 2], acc0[o + 3]};
                             *(LAS f32x4*)(dst + 1024) = f32x4{acc0[o + 4], acc0[o + 5], acc0[o + 6], acc0[o + 7]}; }
                else { *(LAS f32x4*)(dst) = f32x4{acc1[o], acc1[o + 1], acc1[o + 2], acc1[o + 3]};
                       *(LAS f32x4*)(dst + 1024) = f32x4{acc1[o + 4], acc1[o + 5], acc1[o + 6], acc1[o + 7]}; }
            }
        }
        float fin[8];
#pragma unroll
        for (int e = 0; e < 8; ++e) {
            float lo = tq ? acc1[e] : acc0[e], hi = tq ? acc1[8 + e] : acc0[8 + e];
            fin[e] = kh ? hi : lo;
        }
        PIN4(hres); PIN4(x1res);
        LBAR();
#pragma unroll
        for (int sl = 0; sl < 3; ++sl) {
            const LAS char* src = lds + G_SPARE + ((nq * 4 + kq) * 3 + sl) * 2048 + lane * 16;
            f32x4 a0 = *(const LAS f32x4*)(src), a1 = *(const LAS f32x4*)(src + 1024);
#pragma unroll
            for (int e = 0; e < 4; ++e) { fin[e] += a0[e]; fin[4 + e] += a1[e]; }
        }
        u32 o[4];
#pragma unroll
        for (int e = 0; e < 4; ++e) {
            f32x2 h2 = unpk(pk_bf16(fin[2 * e], fin[2 * e + 1]));
            f32x2 t1 = unpk(pk_bf16v(unpk(x1res[e]) + h2));
            o[e] = pk_bf16v(unpk(hres[e]) + t1);
        }
        int tz = threadIdx.x;
        asm volatile("" : "+v"(tz));
        u16* orow = out + (size_t)(tt * 64 + tq * 32 + (tz & 31)) * D + ct * 64 + nq * 32 + kh * 16 + 4 * ((tz >> 5) & 1);
        *(u32x2*)(orow) = u32x2{o[0], o[1]};
        *(u32x2*)(orow + 8) = u32x2{o[2], o[3]};
    }
    TS(25);
    asm volatile(".Lkm_end:");
}

extern "C" void kernel_launch(void* const* d_in, const int* in_sizes, int n_in, void* d_out, int out_size,
                              void* d_ws, size_t ws_size, hipStream_t stream) {
    (void)hipFuncSetAttribute((const void*)k_mega, hipFuncAttributeMaxDynamicSharedMemorySize, LDS_BYTES);
    k_mega<<<256, 512, LDS_BYTES, stream>>>((const u16*)d_in[0], (const u16*)d_in[1], (const u16*)d_in[2],
                                            (const u16*)d_in[3], (const u16*)d_in[4], (const u16*)d_in[5],
                                            (const u16*)d_in[6], (const u16*)d_in[7], (const u16*)d_in[8],
                                            (const u16*)d_in[10], (const u16*)d_in[11], (const u16*)d_in[12],
                                            (const u16*)d_in[13], (u16*)d_out, (char*)d_ws);
}
```

```cpp
#include <hip/hip_runtime.h>
#include <hip/hip_bf16.h>
#include <stdint.h>

#define DEV __device__ __forceinline__
#define LAS __attribute__((address_space(3)))
#define GAS __attribute__((address_space(1)))
typedef unsigned int u32;
typedef unsigned short u16;
typedef unsigned long long u64;
typedef float f32x2 __attribute__((ext_vector_type(2)));
typedef float f32x4 __attribute__((ext_vector_type(4)));
typedef float f32x16 __attribute__((ext_vector_type(16)));
typedef u32 u32x2 __attribute__((ext_vector_type(2)));
typedef u32 u32x4 __attribute__((ext_vector_type(4)));
typedef short s16x4 __attribute__((ext_vector_type(4)));
typedef __bf16 bf16x2 __attribute__((ext_vector_type(2)));
typedef __bf16 bf16x8 __attribute__((ext_vector_type(8)));
typedef _Float16 f16x2 __attribute__((ext_vector_type(2)));
typedef _Float16 f16x8 __attribute__((ext_vector_type(8)));


constexpr int L = 2048, D = 512, NS = 128, F = 1792;

constexpr u32 WS_CTL = 0;
constexpr u32 WS_F1 = 8192;
constexpr u32 WS_F2 = 32768;
constexpr u32 WS_F3 = 65536;
constexpr u32 WS_F4 = 98304;
constexpr u32 WS_F5 = 131072;
constexpr u32 WS_WT3 = 163840;
constexpr u32 WS_WT4 = WS_WT3 + 512 * 1792 * 2;
constexpr u32 WS_HC = WS_WT4 + 512 * 1792 * 2;
constexpr u32 WS_HG = WS_HC + 2048 * 512 * 2;
constexpr u32 WS_X1 = WS_HG + 2048 * 512 * 2;
constexpr u32 WS_G = WS_X1 + 2048 * 512 * 2;
constexpr u32 WS_XU = WS_G + 2048 * 1792 * 2;
constexpr u32 WS_PST = WS_XU + 16384;
constexpr u32 WS_END = WS_PST + 64 * 8192;

__device__ const u32x4 g_zero16[4] = {};

constexpr int c_round_bf16(int Lg) {
    if (Lg < 256) return Lg;
    int e = 0;
    while ((Lg >> (e + 1)) != 0) ++e;
    int sh = e - 7, q = Lg >> sh, rem = Lg & ((1 << sh) - 1), half = 1 << (sh - 1);
    if (rem > half || (rem == half && (q & 1))) ++q;
    return q << sh;
}
constexpr int c_ci(int Lr) {
    if (Lr < 256) return Lr;
    int e = 0;
    while ((Lr >> (e + 1)) != 0) ++e;
    return 128 * (e - 6) + ((Lr >> (e - 7)) - 128);
}
constexpr int KC_ZERO = 644, KC_DUMMY = 648, KC_LEN = 656;
struct Tabs {
    u16 ci[3 * 64 * 4];
    u16 ex[256 * 12];
    constexpr Tabs() : ci(), ex() {
        for (int mt = 0; mt < 3; ++mt)
            for (int l = 0; l < 64; ++l)
                for (int e = 0; e < 4; ++e) {
                    int row = mt * 16 + (l >> 4) * 4 + e, j = l & 15, lrow = 0;
                    if (row < 16) lrow = (row & 7) + 128 * (row >> 3);
                    else if (row < 24) lrow = 256 + 2 * (row & 3) + 128 * ((row >> 2) & 1);
                    else if (row < 32) lrow = 512 + 4 * (row & 1) + 128 * ((row >> 1) & 3);
                    else if (row < 40) lrow = 1024 + 128 * (row & 7);
                    else lrow = 2048;
                    bool valid = row < 40 || (row == 40 && j == 0);
                    ci[(mt * 64 + l) * 4 + e] = (u16)(2 * (valid ? c_ci(lrow + 8 * j) : KC_DUMMY));
                }
        for (int c = 0; c < 256; ++c)
            for (int i = 0; i < 12; ++i) {
                int lag = 2047 - (8 * c + i);
                ex[c * 12 + i] = (u16)(2 * (lag < 0 ? KC_ZERO : c_ci(c_round_bf16(lag))));
            }
    }
};
__device__ const Tabs g_tabs = Tabs();

DEV float bf_lo(u32 v) { return __uint_as_float(v << 16); }
DEV float bf_hi(u32 v) { return __uint_as_float(v & 0xffff0000u); }
DEV float bf1(u16 v) { return __uint_as_float(((u32)v) << 16); }
DEV u32 pk_bf16(float a, float b) {
    f32x2 f = {a, b};
    bf16x2 r = __builtin_convertvector(f, bf16x2);
    return __builtin_bit_cast(u32, r);
}
DEV u32 pk_bf16v(f32x2 f) {
    bf16x2 r = __builtin_convertvector(f, bf16x2);
    return __builtin_bit_cast(u32, r);
}
DEV f32x2 unpk(u32 v) { return f32x2{bf_lo(v), bf_hi(v)}; }
DEV float rbf(float a) { return bf_lo(pk_bf16(a, 0.f)); }
DEV u32 pk_f16(float a, float b) {
    f32x2 f = {a, b};
    f16x2 r = __builtin_convertvector(f, f16x2);
    return __builtin_bit_cast(u32, r);
}
DEV float dot2bf(u32 a, u32 b, float c) {
    return __builtin_amdgcn_fdot2_f32_bf16(__builtin_bit_cast(bf16x2, a), __builtin_bit_cast(bf16x2, b), c, false);
}
DEV f32x16 mfma32(u32x4 a, u32x4 b, f32x16 c) {
    return __builtin_amdgcn_mfma_f32_32x32x16_bf16(__builtin_bit_cast(bf16x8, a), __builtin_bit_cast(bf16x8, b), c, 0, 0, 0);
}
DEV f32x4 mfma16h(u32x4 a, u32x4 b, f32x4 c) {
    return __builtin_amdgcn_mfma_f32_16x16x32_f16(__builtin_bit_cast(f16x8, a), __builtin_bit_cast(f16x8, b), c, 0, 0, 0);
}
template <int CTRL, int RM>
DEV float dpp_f(float v) {
    return __int_as_float(__builtin_amdgcn_update_dpp(0, __float_as_int(v), CTRL, RM, 0xf, true));
}
DEV float wave_sum(float v) {
    v += dpp_f<0x111, 0xf>(v); v += dpp_f<0x112, 0xf>(v); v += dpp_f<0x114, 0xf>(v); v += dpp_f<0x118, 0xf>(v);
    v += dpp_f<0x142, 0xa>(v); v += dpp_f<0x143, 0xc>(v);
    return __int_as_float(__builtin_amdgcn_readlane(__float_as_int(v), 63));
}
DEV float add_xor32(float v) {
    u32 u = __float_as_uint(v);
    auto sw = __builtin_amdgcn_permlane32_swap(u, u, false, false);
    u32 a = sw[0], b = sw[1];
    return __uint_as_float(a) + __uint_as_float(b);
}
#define WAIT_V(n) asm volatile("s_waitcnt vmcnt(" #n ")" ::: "memory")
#define FENCE() asm volatile("" ::: "memory")
#define LBAR() do { asm volatile("s_waitcnt lgkmcnt(0)" ::: "memory"); __builtin_amdgcn_s_barrier(); asm volatile("" ::: "memory"); } while (0)
#define PIN4(a) asm volatile("" : "+v"(a))

DEV void ln_consts(float mean, float var, float& m_bf, float& sd_bf) {
    m_bf = rbf(mean);
    float v = rbf(var);
    v = rbf(v + 1e-5f);
    sd_bf = rbf(__builtin_amdgcn_sqrtf(v));
}
DEV void dma_ws2(u32x4 srd, u32 voff, u32 soff, u32 ldst) {
    asm volatile("s_mov_b32 m0, %0\n\ts_nop 0\n\tbuffer_load_dwordx4 %1, %2, %3 offen sc1 lds\n\t"
                 "buffer_load_dwordx4 %1, %2, %3 offen offset:1024 sc1 lds"
                 :: "s"(ldst), "v"(voff), "s"(srd), "s"(soff) : "memory");
}
DEV void dma_ws4(u32x4 srd, u32 voff, u32 soff, u32 ldst) {
    asm volatile("s_mov_b32 m0, %0\n\ts_nop 0\n\tbuffer_load_dwordx4 %1, %2, %3 offen sc1 lds\n\t"
                 "buffer_load_dwordx4 %1, %2, %3 offen offset:1024 sc1 lds\n\t"
                 "buffer_load_dwordx4 %1, %2, %3 offen offset:2048 sc1 lds\n\t"
                 "buffer_load_dwordx4 %1, %2, %3 offen offset:3072 sc1 lds"
                 :: "s"(ldst), "v"(voff), "s"(srd), "s"(soff) : "memory");
}
DEV void dma_in(u32x4 srd, u32 voff, u32 soff, u32 ldst) {
    asm volatile("s_mov_b32 m0, %0\n\ts_nop 0\n\tbuffer_load_dwordx4 %1, %2, %3 offen lds"
                 :: "s"(ldst), "v"(voff), "s"(srd), "s"(soff) : "memory");
}
DEV void dma_g(const void* g, u32 ldst) {
    asm volatile("s_mov_b32 m0, %0\n\ts_nop 0\n\tglobal_load_lds_dwordx4 %1, off sc1"
                 :: "s"(ldst), "v"(g) : "memory");
}
DEV u32x4 make_srd(const void* p, u32 bytes) {
    u64 a = (u64)p;
    return u32x4{(u32)a, (u32)(a >> 32) & 0xffffu, bytes, 0x00020000u};
}
#define SCHED0() __builtin_amdgcn_sched_barrier(0)
DEV u32x4 tr_frag(const LAS char* p) {
    s16x4 a = __builtin_amdgcn_ds_read_tr16_b64_v4i16((LAS s16x4*)p);
    s16x4 b = __builtin_amdgcn_ds_read_tr16_b64_v4i16((LAS s16x4*)(p + 1024));
    u32x2 x = __builtin_bit_cast(u32x2, a), y = __builtin_bit_cast(u32x2, b);
    return u32x4{x[0], x[1], y[0], y[1]};
}

struct cplx { float x, y; };
DEV cplx cmul(cplx a, cplx b) {
    cplx r;
    asm("v_mul_f32 %0, %3, %5\n\tv_mul_f32 %1, %3, %4\n\tv_fma_f32 %0, %2, %4, -%0\n\tv_fma_f32 %1, %2, %5, %1"
        : "=&v"(r.x), "=&v"(r.y) : "v"(a.x), "v"(a.y), "v"(b.x), "v"(b.y));
    return r;
}
DEV cplx csq(cplx a) {
    cplx r;
    asm("v_mul_f32 %0, %3, %3\n\tv_mul_f32 %1, %2, %3\n\tv_fma_f32 %0, %2, %2, -%0\n\tv_add_f32 %1, %1, %1"
        : "=&v"(r.x), "=&v"(r.y) : "v"(a.x), "v"(a.y));
    return r;
}
DEV cplx cexp_pow2(float tr, float wh, float wl, float p2) {
    float mag = __builtin_amdgcn_exp2f(tr * p2);
    float ph = __builtin_amdgcn_fractf(wh * p2) + wl * p2;
    return cplx{mag * __builtin_amdgcn_cosf(ph), mag * __builtin_amdgcn_sinf(ph)};
}
template <int CTRL, int RM>
DEV u32 dpp_u(u32 v) { return (u32)__builtin_amdgcn_update_dpp(0, (int)v, CTRL, RM, 0xf, true); }
DEV u32 wave_max_u(u32 v) {
    v = max(v, dpp_u<0x111, 0xf>(v)); v = max(v, dpp_u<0x112, 0xf>(v)); v = max(v, dpp_u<0x114, 0xf>(v));
    v = max(v, dpp_u<0x118, 0xf>(v)); v = max(v, dpp_u<0x142, 0xa>(v)); v = max(v, dpp_u<0x143, 0xc>(v));
    return (u32)__builtin_amdgcn_readlane((int)v, 63);
}
DEV float gelu_s(float d, float r, float ga, float gb) {
    float t = d * __builtin_fmaf(d * d, gb, ga);
    float e = __builtin_amdgcn_exp2f(t);
    return (d * r) * __builtin_amdgcn_rcpf(1.0f + e);
}
constexpr float GC1 = -2.0f * 0.7978845608028654f * 1.4426950408889634f, GC3 = GC1 * 0.044715f;

constexpr int TRS = 528;
constexpr int RCOPY = 4160;
constexpr int A_R = 0;
constexpr int A_U = 33280, UPAD = 240, ULEN = 2304;
constexpr int A_KC = A_U + 2 * ULEN * 2;
constexpr int A_X = 45312, A_XCH = 41 * TRS;
constexpr int A_Y = A_X + 2 * A_XCH, A_YCH = 16 * TRS;
constexpr int A_SCR = 107520;
constexpr int A_YP = 45312;
constexpr int A_W = 98304;
constexpr int LDS_BYTES = 163840;
constexpr int G_ACT = 0, G_RED = 65536, G_PS = 81920, G_CS = 86016, G_SPARE = 114688;
constexpr int SPIN_MAX = 1 << 18;

__global__ __launch_bounds__(512, 2) void k_mega(const u16* __restrict__ x, const u16* __restrict__ A_re,
                                                 const u16* __restrict__ A_im, const u16* __restrict__ B_re,
                                                 const u16* __restrict__ B_im, const u16* __restrict__ C_re,
                                                 const u16* __restrict__ C_im, const u16* __restrict__ log_dt,
                                                 const u16* __restrict__ Dskip, const u16* __restrict__ w1,
                                                 const u16* __restrict__ w2, const u16* __restrict__ w3,
                                                 const u16* __restrict__ w4, u16* __restrict__ out, char* ws) {
    extern __shared__ __attribute__((aligned(16))) char lds_raw[];
    LAS char* lds = (LAS char*)lds_raw;
    const int tid = threadIdx.x, lane = tid & 63, wave = __builtin_amdgcn_readfirstlane(tid >> 6);
    const int b = blockIdx.x, bq = b >> 3;
    const int cb = (b & 7) * 32 + bq, d0 = cb * 2;
    const int tt = (b & 7) * 4 + (bq >> 3), ct = bq & 7;
    const auto rs = __builtin_amdgcn_make_buffer_rsrc(ws, 0, WS_END, 0x00020000);
    const u32x4 srd_ws = make_srd(ws, WS_END), srd_w1 = make_srd(w1, D * D * 2), srd_w2 = make_srd(w2, D * D * 2);
    const u32 lbase = (u32)(size_t)lds;
    asm volatile("" :: "s"(x), "s"(A_re), "s"(A_im), "s"(B_re), "s"(B_im), "s"(C_re), "s"(C_im), "s"(log_dt), "s"(Dskip),
                 "s"(w1), "s"(w2), "s"(w3), "s"(w4), "s"(out), "s"(ws));

    const u32 ep = *(const GAS u32*)(ws + WS_CTL) + 1u;
    u32x4 xr = *(const u32x4*)(x + (size_t)(b * 8 + wave) * D + lane * 8);
    const int kch = wave >> 2, knh = wave & 1, kpart = (wave >> 1) & 1;
    const int pidx = (d0 + kch) * NS + knh * 64 + lane;
    u16 p_are = A_re[pidx], p_aim = A_im[pidx], p_bre = B_re[pidx], p_bim = B_im[pidx], p_cre = C_re[pidx], p_cim = C_im[pidx];
    u16 o_bre = B_re[pidx ^ 64], o_bim = B_im[pidx ^ 64], o_cre = C_re[pidx ^ 64], o_cim = C_im[pidx ^ 64];
    u32 p_ldt = *(const u32*)(log_dt + d0);
    u32 p_dsk = *(const u32*)(Dskip + d0);
    u32x4 traw[2];
    u32 tdst = 0;
    {
        int pr = b * 7 + (wave < 7 ? wave : 0);
        const u16* src; int N;
        if (pr < 896) { int ntp = pr >> 5, ks = pr & 31; src = w3 + (size_t)(16 * ks) * F + ntp * 64; N = F;
                        tdst = WS_WT3 + ((ntp * 2) * 32 + ks) * 1024; }
        else { int p2 = pr - 896; int ntp = p2 / 112, ks = p2 - ntp * 112; src = w4 + (size_t)(16 * ks) * D + ntp * 64; N = D;
               tdst = WS_WT4 + ((ntp * 2) * 112 + ks) * 1024; }
#pragma unroll
        for (int i = 0; i < 2; ++i) traw[i] = *(const u32x4*)(src + (size_t)(8 * i + (lane >> 3)) * N + (lane & 7) * 8);
    }
    u32x2 tci = *(const u32x2*)(g_tabs.ci + ((wave & 3) < 3 ? (wave & 3) : 0) * 256 + lane * 4);
    u32x4 tex0 = *(const u32x4*)(g_tabs.ex + (tid & 255) * 12);
    u32x2 tex1 = *(const u32x2*)(g_tabs.ex + (tid & 255) * 12 + 8);

    FENCE();
    __builtin_amdgcn_s_barrier();
    u32 xv[4];
    const u16* xcol = x + (size_t)(tid * 4) * D + d0;
#define GATHER(k) do { FENCE(); xv[k] = *(const u32*)(xcol + (k) * D); FENCE(); } while (0)
    GATHER(0);
    const int trq = (lane & 15) >> 2, trp = lane & 3, trh = lane >> 5, trg = (lane >> 4) & 1;
    const int tr_off = (4 * trh + trq) * 128 + (((2 * trg + (trp >> 1)) ^ ((trq >> 1) << 2)) << 4) + 8 * (trp & 1);
    const int dma_sw = ((lane & 7) ^ (((lane >> 4) & 1) << 2)) * 8;
    {
        f32x2 f[4];
#pragma unroll
        for (int i = 0; i < 4; ++i) f[i] = unpk(xr[i]);
        f32x2 s2 = (f[0] + f[1]) + (f[2] + f[3]);
        float mean = wave_sum(s2.x + s2.y) * (1.0f / D);
        f32x2 q2 = {0.f, 0.f};
#pragma unroll
        for (int i = 0; i < 4; ++i) { f32x2 dd = f[i] - mean; q2 += dd * dd; }
        float var = wave_sum(q2.x + q2.y) * (1.0f / D);
        float m, sd;
        ln_consts(mean, var, m, sd);
        const u32 val = (__float_as_uint(m) >> 16) | (__float_as_uint(sd) & 0xffff0000u);
        if (lane == 0)
            __hip_atomic_store((GAS u64*)(ws + WS_XU) + (b * 8 + wave), ((u64)ep << 32) | val, __ATOMIC_RELAXED, __HIP_MEMORY_SCOPE_AGENT);
        GATHER(1);
        LAS char* scr = lds + A_SCR + wave * 2048;
#pragma unroll
        for (int i = 0; i < 2; ++i)
            *(LAS u32x4*)(scr + i * 1024 + (lane >> 3) * 128 + (((lane & 7) ^ (((lane >> 4) & 1) << 2)) << 4)) = traw[i];
        u32x4 f0 = tr_frag(scr + tr_off), f1 = tr_frag(scr + (tr_off ^ 64));
        if (wave < 7) {
            const u32 nstride = tdst < WS_WT4 ? 32 * 1024 : 112 * 1024;
            __builtin_amdgcn_raw_buffer_store_b128(f0, rs, tdst + lane * 16, 0, 16);
            __builtin_amdgcn_raw_buffer_store_b128(f1, rs, tdst + nstride + lane * 16, 0, 16);
        }
        FENCE();
    }
    float inv_scale[2];
    {
        float dts[2] = {rbf(__expf(bf_lo(p_ldt))), rbf(__expf(bf_hi(p_ldt)))};
        float dt = kch ? dts[1] : dts[0];
        float zr = dt * bf1(p_are), zi = dt * bf1(p_aim);
        float tr = zr * 1.4426950408889634f;
        const float inv2pi_h = 0.15915494309189535f;
        const float inv2pi_l = (float)(0.15915494309189533576888 - (double)inv2pi_h);
        float wh = zi * inv2pi_h;
        float wl = __builtin_fmaf(zi, inv2pi_h, -wh) + zi * inv2pi_l;
        cplx CB = cmul(cplx{bf1(p_cre), bf1(p_cim)}, cplx{dt * bf1(p_bre), dt * bf1(p_bim)});
        cplx CO = cmul(cplx{bf1(o_cre), bf1(o_cim)}, cplx{dt * bf1(o_bre), dt * bf1(o_bim)});
        u32 mx = wave_max_u(max(max(__float_as_uint(CB.x) & 0x7fffffffu, __float_as_uint(CB.y) & 0x7fffffffu),
                                max(__float_as_uint(CO.x) & 0x7fffffffu, __float_as_uint(CO.y) & 0x7fffffffu)));
        int e = (int)(mx >> 23);
        e = e < 8 ? 8 : (e > 246 ? 246 : e);
        float sc = __uint_as_float((u32)(254 + 6 - e) << 23);
        CB.x *= sc; CB.y *= sc;
        if (lane == 0 && knh == 0 && kpart == 0) {
            *(LAS float*)(lds + A_KC + (kch * KC_LEN + 652) * 2) = __uint_as_float((u32)(e - 6) << 23);
            *(LAS u32*)(lds + A_KC + (kch * KC_LEN + KC_ZERO) * 2) = 0u;
        }
        GATHER(2);
        LAS char* Xb = lds + A_X + kch * A_XCH + (knh * 64 + lane) * 4;
        LAS char* Yb = lds + A_Y + kch * A_YCH + (knh * 64 + lane) * 4;
#define PUTX(row, v) *(LAS u32*)(Xb + (row) * TRS) = pk_f16((v).x, (v).y)
#define PUTY(row, v) *(LAS u32*)(Yb + (row) * TRS) = pk_f16((v).x, (v).y)
        if (kpart == 0) {
            cplx P1 = cexp_pow2(tr, wh, wl, 1.f), Q8 = cexp_pow2(tr, -wh, -wl, 8.f), P128 = cexp_pow2(tr, wh, wl, 128.f);
            cplx P2 = csq(P1), Q16 = csq(Q8), P4 = csq(P2), Q32 = csq(Q16), Q64 = csq(Q32);
            cplx Y[8];
            Y[0] = cplx{1.f, 0.f}; Y[1] = Q8; Y[2] = Q16; Y[3] = cmul(Q8, Q16); Y[4] = Q32;
            Y[5] = cmul(Q32, Q8); Y[6] = cmul(Q32, Q16); Y[7] = cmul(Q32, Y[3]);
#pragma unroll
            for (int j = 0; j < 8; ++j) PUTY(j, Y[j]);
            PUTY(8, Q64);
#pragma unroll
            for (int j = 1; j < 8; ++j) { cplx v = cmul(Q64, Y[j]); PUTY(8 + j, v); }
            cplx Xa[8];
            Xa[0] = CB; Xa[1] = cmul(CB, P1); Xa[2] = cmul(CB, P2); Xa[3] = cmul(Xa[2], P1);
#pragma unroll
            for (int j = 0; j < 4; ++j) Xa[4 + j] = cmul(Xa[j], P4);
#pragma unroll
            for (int j = 0; j < 8; ++j) { PUTX(j, Xa[j]); cplx v = cmul(Xa[j], P128); PUTX(8 + j, v); }
        } else {
            cplx P2 = cexp_pow2(tr, wh, wl, 2.f), P128 = cexp_pow2(tr, wh, wl, 128.f), P1024 = cexp_pow2(tr, wh, wl, 1024.f);
            cplx P4 = csq(P2), P256 = csq(P128), P2048 = csq(P1024), P512 = csq(P256);
            cplx X0 = CB, X2 = cmul(CB, P2), X4 = cmul(CB, P4), X6 = cmul(X2, P4);
            cplx X128 = cmul(CB, P128), X132 = cmul(X4, P128);
            cplx c0[4] = {cmul(X0, P256), cmul(X2, P256), cmul(X4, P256), cmul(X6, P256)};
            cplx c1[4];
#pragma unroll
            for (int j = 0; j < 4; ++j) c1[j] = cmul(c0[j], P128);
#pragma unroll
            for (int j = 0; j < 4; ++j) { PUTX(16 + j, c0[j]); PUTX(20 + j, c1[j]); }
            cplx dd[8] = {cmul(X0, P512), cmul(X4, P512), cmul(X128, P512), cmul(X132, P512),
                          cmul(c0[0], P512), cmul(c0[2], P512), cmul(c1[0], P512), cmul(c1[2], P512)};
#pragma unroll
            for (int j = 0; j < 8; ++j) PUTX(24 + j, dd[j]);
            cplx ee[8] = {X0, X128, c0[0], c1[0], dd[0], dd[2], dd[4], dd[6]};
#pragma unroll
            for (int j = 0; j < 8; ++j) { cplx v = cmul(ee[j], P1024); PUTX(32 + j, v); }
            cplx v = cmul(CB, P2048);
            PUTX(40, v);
        }
    }

    LBAR();
    GATHER(3);
    if ((wave & 3) < 3) {
        const int mt = wave & 3, r = lane & 15, g = lane >> 4;
        const LAS char* Xb = lds + A_X + kch * A_XCH + (mt * 16 + r) * TRS + g * 16;
        const LAS char* Yb = lds + A_Y + kch * A_YCH + r * TRS + g * 16;
        f32x4 acc0 = {0, 0, 0, 0}, acc1 = {0, 0, 0, 0};
#pragma unroll
        for (int ks = 0; ks < 8; ks += 2) {
            acc0 = mfma16h(*(const LAS u32x4*)(Xb + ks * 64), *(const LAS u32x4*)(Yb + ks * 64), acc0);
            acc1 = mfma16h(*(const LAS u32x4*)(Xb + ks * 64 + 64), *(const LAS u32x4*)(Yb + ks * 64 + 64), acc1);
        }
        acc0 += acc1;
        u32 k01 = pk_f16(acc0[0], acc0[1]), k23 = pk_f16(acc0[2], acc0[3]);
        LAS char* Kc = lds + A_KC + kch * KC_LEN * 2;
        *(LAS u16*)(Kc + (tci[0] & 0xffff)) = (u16)k01;
        *(LAS u16*)(Kc + (tci[0] >> 16)) = (u16)(k01 >> 16);
        *(LAS u16*)(Kc + (tci[1] & 0xffff)) = (u16)k23;
        *(LAS u16*)(Kc + (tci[1] >> 16)) = (u16)(k23 >> 16);
    }
    u32x4 g0 = __builtin_amdgcn_raw_buffer_load_b128(rs, WS_XU + tid * 32, 0, 16);
    u32x4 g1 = __builtin_amdgcn_raw_buffer_load_b128(rs, WS_XU + tid * 32 + 16, 0, 16);
    FENCE();
    LBAR();
    {
        const int ch = tid >> 8, c = tid & 255;
        const LAS char* Kc = lds + A_KC + ch * KC_LEN * 2;
        u32 pe[6];
        u32 tw[6] = {tex0[0], tex0[1], tex0[2], tex0[3], tex1[0], tex1[1]};
#pragma unroll
        for (int i = 0; i < 6; ++i) {
            u32 lo = *(const LAS u16*)(Kc + (tw[i] & 0xffff));
            u32 hi = *(const LAS u16*)(Kc + (tw[i] >> 16));
            pe[i] = lo | (hi << 16);
        }
        u32 po[5];
#pragma unroll
        for (int i = 0; i < 5; ++i) po[i] = __builtin_amdgcn_alignbit(pe[i + 1], pe[i], 16);
        inv_scale[0] = *(const LAS float*)(lds + A_KC + 652 * 2);
        inv_scale[1] = *(const LAS float*)(lds + A_KC + (KC_LEN + 652) * 2);
        LAS char* Rb = lds + A_R + ch * 4 * RCOPY + c * 16;
        *(LAS u32x4*)(Rb + 0 * RCOPY) = u32x4{pe[0], pe[1], pe[2], pe[3]};
        *(LAS u32x4*)(Rb + 1 * RCOPY) = u32x4{po[0], po[1], po[2], po[3]};
        *(LAS u32x4*)(Rb + 2 * RCOPY) = u32x4{pe[1], pe[2], pe[3], pe[4]};
        *(LAS u32x4*)(Rb + 3 * RCOPY) = u32x4{po[1], po[2], po[3], po[4]};
        if (tid < 16) *(LAS u32x4*)(lds + A_R + (tid >> 1) * RCOPY + 4096 + (tid & 1) * 16) = u32x4{0, 0, 0, 0};
        if (tid >= 64 && tid < 124) {
            *(LAS u32x2*)(lds + A_U + (tid - 64) * 8) = u32x2{0, 0};
            *(LAS u32x2*)(lds + A_U + ULEN * 2 + (tid - 64) * 8) = u32x2{0, 0};
        }
    }

    _Pragma("unroll 1") for (int spin = 0; spin < SPIN_MAX; ++spin) {
        const bool ok = g0[1] == ep && g0[3] == ep && g1[1] == ep && g1[3] == ep;
        if (__all(ok)) break;
        __builtin_amdgcn_s_sleep(2);
        FENCE();
        g0 = __builtin_amdgcn_raw_buffer_load_b128(rs, WS_XU + tid * 32, 0, 16);
        g1 = __builtin_amdgcn_raw_buffer_load_b128(rs, WS_XU + tid * 32 + 16, 0, 16);
    }
    u32 udp[4];
    {
        const u32 st[4] = {g0[0], g0[2], g1[0], g1[2]};
        const f32x2 dsk = unpk(p_dsk);
        f32x2 u[4];
#pragma unroll
        for (int k = 0; k < 4; ++k) {
            const float m = bf_lo(st[k]), rsd = __builtin_amdgcn_rcpf(bf_hi(st[k]));
            const f32x2 dlt = unpk(pk_bf16v(unpk(xv[k]) - m));
            u[k] = unpk(pk_bf16v(dlt * rsd));
            udp[k] = pk_bf16v(u[k] * dsk);
        }
        *(LAS u32x2*)(lds + A_U + (UPAD + tid * 4) * 2) = u32x2{pk_f16(u[0].x, u[1].x), pk_f16(u[2].x, u[3].x)};
        *(LAS u32x2*)(lds + A_U + ULEN * 2 + (UPAD + tid * 4) * 2) = u32x2{pk_f16(u[0].y, u[1].y), pk_f16(u[2].y, u[3].y)};
    }
    FENCE();
    u32 ww0 = 0, ww1 = 0;
    if (wave == 2 || wave == 3) {
        const u32 wo = (bq * 128 + lane) * 128;
        if (wave == 2) {
            asm volatile("buffer_load_dword %0, %1, %2, 0 offen" : "=v"(ww0) : "v"(wo), "s"(srd_w1) : "memory");
            asm volatile("buffer_load_dword %0, %1, %2, 0 offen" : "=v"(ww1) : "v"(wo + 8192), "s"(srd_w1) : "memory");
        } else {
            asm volatile("buffer_load_dword %0, %1, %2, 0 offen" : "=v"(ww0) : "v"(wo), "s"(srd_w2) : "memory");
            asm volatile("buffer_load_dword %0, %1, %2, 0 offen" : "=v"(ww1) : "v"(wo + 8192), "s"(srd_w2) : "memory");
        }
    }
    const u32 w3off = wave < 7 ? WS_WT3 + (ct * 7 + wave) * 32768 + lane * 16 : 0xF0000000u;
    u32x4 w3f[32];
    float c3a = 0.f, c3b = 0.f;
#define CS3(lo, hi) do { _Pragma("unroll") for (int i_ = (lo); i_ < (hi); ++i_) { \
        c3a = dot2bf(w3f[i_][0], 0x3f803f80u, c3a); c3b = dot2bf(w3f[i_][1], 0x3f803f80u, c3b); \
        c3a = dot2bf(w3f[i_][2], 0x3f803f80u, c3a); c3b = dot2bf(w3f[i_][3], 0x3f803f80u, c3b); } } while (0)
#define W3LOAD(lo, hi) do { FENCE(); _Pragma("unroll") for (int i_ = (lo); i_ < (hi); ++i_) \
        w3f[i_] = __builtin_amdgcn_raw_buffer_load_b128(rs, w3off + i_ * 1024, 0, 16); FENCE(); } while (0)
    const u32 img_voff = (lane >> 3) * (D * 2) + ct * 128 + dma_sw * 2;
    LBAR();
    {
        const int ch = wave >> 2, rq = wave & 3;
        const int r = lane & 15, g = lane >> 4;
        const int o0 = 15 - r + 8 * g;
        const LAS char* Ab = lds + A_R + ch * 4 * RCOPY + (o0 & 3) * RCOPY + (o0 & ~3) * 2 + (6 - rq * 2) * 64;
        const LAS char* Bb = lds + A_U + ch * ULEN * 2 + (16 * r + 8 * g) * 2 + (6 - rq * 2) * 64;
        f32x4 acc[8];
#pragma unroll
        for (int i = 0; i < 8; ++i) acc[i] = f32x4{0, 0, 0, 0};
        u32x4 A0[8], B0[8], A1[8], B1[8];
#define LD1(AF, BF, a, rr) do { \
            u32x2 lo_ = *(const volatile LAS u32x2*)(Ab + (1 - (rr)) * 64 + (7 - (a)) * 512); \
            u32x2 hi_ = *(const volatile LAS u32x2*)(Ab + (1 - (rr)) * 64 + (7 - (a)) * 512 + 8); \
            AF[a] = u32x4{lo_[0], lo_[1], hi_[0], hi_[1]}; \
            BF[a] = *(const LAS u32x4*)(Bb + (1 - (rr)) * 64 + (a) * 512); } while (0)
#define GRP(AF, BF, m) do { _Pragma("unroll") for (int o_ = 0; o_ <= (m); ++o_) if (o_ + (m) <= 7) { \
            acc[o_ + (m)] = mfma16h(AF[m], BF[o_], acc[o_ + (m)]); \
            if (o_ != (m)) acc[o_ + (m)] = mfma16h(AF[o_], BF[m], acc[o_ + (m)]); } } while (0)
        LD1(A0, B0, 0, 0); LD1(A0, B0, 7, 0); LD1(A0, B0, 1, 0); LD1(A0, B0, 6, 0);
        SCHED0();
        GRP(A0, B0, 7); LD1(A0, B0, 2, 0); LD1(A0, B0, 5, 0); SCHED0();
        GRP(A0, B0, 6); LD1(A0, B0, 3, 0); LD1(A0, B0, 4, 0); SCHED0();
        GRP(A0, B0, 5); SCHED0();
        GRP(A0, B0, 4); LD1(A1, B1, 0, 1); SCHED0();
        GRP(A0, B0, 3); LD1(A1, B1, 1, 1); SCHED0();
        GRP(A0, B0, 2); LD1(A1, B1, 2, 1); SCHED0();
        GRP(A0, B0, 1); LD1(A1, B1, 3, 1); SCHED0();
        GRP(A0, B0, 0); GRP(A1, B1, 0); LD1(A1, B1, 4, 1); SCHED0();
        GRP(A1, B1, 1); LD1(A1, B1, 5, 1); SCHED0();
        GRP(A1, B1, 2); LD1(A1, B1, 6, 1); SCHED0();
        GRP(A1, B1, 3); LD1(A1, B1, 7, 1); SCHED0();
        GRP(A1, B1, 4); SCHED0();
        GRP(A1, B1, 5); SCHED0();
        GRP(A1, B1, 6); SCHED0();
        GRP(A1, B1, 7);
        LAS char* Yp = lds + A_YP + (rq * 2 + ch) * 8192 + g * 256 + ((r ^ (4 * g) ^ ch) << 4);
#pragma unroll
        for (int c = 0; c < 8; ++c) *(LAS f32x4*)(Yp + 1024 * c) = acc[c];
    }
    LBAR();
    {
        const int sl = (lane >> 2) ^ (4 * (lane & 3));
        const LAS char* Y0 = lds + A_YP + wave * 1024 + (lane & 3) * 256 + (sl << 4);
        const LAS char* Y1 = lds + A_YP + 8192 + wave * 1024 + (lane & 3) * 256 + ((sl ^ 1) << 4);
        f32x4 y0 = ((*(const LAS f32x4*)(Y0) + *(const LAS f32x4*)(Y0 + 16384)) +
                    (*(const LAS f32x4*)(Y0 + 32768) + *(const LAS f32x4*)(Y0 + 49152))) * inv_scale[0];
        f32x4 y1 = ((*(const LAS f32x4*)(Y1) + *(const LAS f32x4*)(Y1 + 16384)) +
                    (*(const LAS f32x4*)(Y1 + 32768) + *(const LAS f32x4*)(Y1 + 49152))) * inv_scale[1];
        u32 hp[4];
#pragma unroll
        for (int k = 0; k < 4; ++k) {
            f32x2 yy = unpk(pk_bf16(y0[k], y1[k]));
            f32x2 a = unpk(pk_bf16v(yy + unpk(udp[k])));
            hp[k] = pk_bf16v(unpk(xv[k]) + a);
        }
        u32 c00 = __builtin_amdgcn_perm(hp[1], hp[0], 0x05040100), c01 = __builtin_amdgcn_perm(hp[3], hp[2], 0x05040100);
        u32 c10 = __builtin_amdgcn_perm(hp[1], hp[0], 0x07060302), c11 = __builtin_amdgcn_perm(hp[3], hp[2], 0x07060302);
        const bool odd = lane & 1;
        u32 s0 = odd ? c00 : c10, s1 = odd ? c01 : c11;
        u32 r0 = __builtin_amdgcn_update_dpp(0, s0, 0xB1, 0xf, 0xf, true);
        u32 r1 = __builtin_amdgcn_update_dpp(0, s1, 0xB1, 0xf, 0xf, true);
        u32x4 o = odd ? u32x4{r0, r1, c10, c11} : u32x4{c00, c01, r0, r1};
        __builtin_amdgcn_raw_buffer_store_b128(o, rs, WS_HC + (tid >> 4) * 65536 + (d0 + (odd ? 1 : 0)) * 128 + ((tid >> 1) & 7) * 16, 0, 16);
    }
    LBAR();
#pragma unroll
    for (int i = 0; i < 8; ++i) dma_in(srd_w1, img_voff, (wave * 8 + i) * 8 * D * 2, lbase + G_ACT + (wave * 8 + i) * 1024);
    WAIT_V(8);
    LBAR();
    if (tid < 8) __hip_atomic_store((GAS u32*)(ws + WS_F1) + tid * 256 + b, ep, __ATOMIC_RELAXED, __HIP_MEMORY_SCOPE_AGENT);
    asm volatile("" :: "v"(ww0), "v"(ww1));
#pragma unroll
    for (int i = 0; i < 8; ++i) dma_in(srd_w2, img_voff, (wave * 8 + i) * 8 * D * 2, lbase + A_W + (wave * 8 + i) * 1024);
    WAIT_V(8);
    LBAR();

    const int tq = wave & 1, nq = (wave >> 1) & 1, kh = wave >> 2;
    const int r32 = lane & 31, h32 = lane >> 5;
    const u32 ones = 0x3f803f80u;
    u32x4 wf[32];
    {
        const LAS char* WbE = lds + G_ACT + kh * 8192 + (tr_off ^ (nq << 6)) + nq * 2048;
        const LAS char* WbO = WbE - nq * 4096;
#pragma unroll
        for (int i = 0; i < 16; ++i) wf[i] = tr_frag(((i & 1) ? WbO : WbE) + ((i >> 2) * 8 + (i & 3)) * 2048);
    }
    if (wave == 7) {
        _Pragma("unroll 1") for (int spin = 0; spin < SPIN_MAX; ++spin) {
            FENCE();
            u32x4 f = __builtin_amdgcn_raw_buffer_load_b128(rs, WS_F1 + (b & 7) * 1024 + lane * 16, 0, 16);
            if (__all(f[0] == ep && f[1] == ep && f[2] == ep && f[3] == ep)) break;
            __builtin_amdgcn_s_sleep(1);
        }
        if (b == 0 && lane == 0) *(GAS u32*)(ws + WS_CTL) = ep;
    }
    LBAR();
    {
        const u32 voff = (lane >> 3) * 128 + dma_sw * 2;
#pragma unroll
        for (int c = 0; c < 4; ++c)
            dma_ws2(srd_ws, voff, WS_HC + tt * 65536 + (8 * c + wave) * 2048, lbase + G_ACT + (8 * c + wave) * 2048);
    }
    {
        float ca = 0.f, cb_ = 0.f;
#pragma unroll
        for (int i = 0; i < 16; ++i) {
            ca = dot2bf(wf[i][0], ones, ca); cb_ = dot2bf(wf[i][1], ones, cb_);
            ca = dot2bf(wf[i][2], ones, ca); cb_ = dot2bf(wf[i][3], ones, cb_);
        }
        const float csum = add_xor32(ca + cb_);
        if (h32 == 0 && tq == 0) *(LAS float*)(lds + G_CS + ((nq * 2 + kh) * 32 + r32) * 4) = csum;
    }
    u32x4 hres, x1res;
    {
        f32x16 acc = {};
        u32x4 own[2];
        float s1a = 0.f, s1b = 0.f, q1a = 0.f, q1b = 0.f;
        const LAS char* Ab = lds + G_ACT + (tr_off ^ (tq << 6));
        const LAS char* AbE = Ab + kh * 8192 + nq * 2048;
        const LAS char* AbO = AbE - nq * 4096;
#pragma unroll
        for (int c = 0; c < 4; ++c) {
            if (c == 0) WAIT_V(6); else if (c == 1) WAIT_V(4); else if (c == 2) WAIT_V(2); else WAIT_V(0);
            LBAR();
#pragma unroll
            for (int kk = 0; kk < 4; ++kk) {
                u32x4 bb = tr_frag(((kk & 1) ? AbO : AbE) + (8 * c + kk) * 2048);
                acc = mfma32(wf[4 * c + kk], bb, acc);
                if ((kk & 1) == 0) own[kk >> 1] = bb;
            }
#pragma unroll
            for (int i = 0; i < 2; ++i) {
                const u32x4 v = own[i];
                s1a = dot2bf(v[0], ones, s1a); s1b = dot2bf(v[1], ones, s1b);
                q1a = dot2bf(v[0], v[0], q1a); q1b = dot2bf(v[1], v[1], q1b);
                s1a = dot2bf(v[2], ones, s1a); s1b = dot2bf(v[3], ones, s1b);
                q1a = dot2bf(v[2], v[2], q1a); q1b = dot2bf(v[3], v[3], q1b);
            }
        }
        hres = tr_frag(Ab + (4 * ct + 2 * nq + kh) * 2048);
        W3LOAD(0, 8);
        {
            LAS char* p = lds + G_RED + wave * 2048 + lane * 16;
            if (kh == 0) { *(LAS f32x4*)(p) = f32x4{acc[8], acc[9], acc[10], acc[11]}; *(LAS f32x4*)(p + 1024) = f32x4{acc[12], acc[13], acc[14], acc[15]}; }
            else { *(LAS f32x4*)(p) = f32x4{acc[0], acc[1], acc[2], acc[3]}; *(LAS f32x4*)(p + 1024) = f32x4{acc[4], acc[5], acc[6], acc[7]}; }
        }
        float s1 = add_xor32(s1a + s1b), q1 = add_xor32(q1a + q1b);
        LAS char* ps = lds + G_PS + (tq * 32 + r32) * 32;
        if (h32 == 0) *(LAS f32x2*)(ps + (nq * 2 + kh) * 8) = f32x2{s1, q1};
        LBAR();
    float csv[8];
    {
        const LAS char* p = lds + G_CS + nq * 256 + (16 * kh + 4 * h32) * 4;
        f32x4 a0 = *(const LAS f32x4*)(p), a1 = *(const LAS f32x4*)(p + 32);
        f32x4 b0 = *(const LAS f32x4*)(p + 128), b1 = *(const LAS f32x4*)(p + 160);
#pragma unroll
        for (int e = 0; e < 4; ++e) { csv[e] = a0[e] + b0[e]; csv[4 + e] = a1[e] + b1[e]; }
    }
        float fin[8];
        {
            const LAS char* p = lds + G_RED + (wave ^ 4) * 2048 + lane * 16;
            f32x4 a0 = *(const LAS f32x4*)(p), a1 = *(const LAS f32x4*)(p + 1024);
#pragma unroll
            for (int e = 0; e < 4; ++e) {
                fin[e] = a0[e] + (kh ? acc[8 + e] : acc[e]);
                fin[4 + e] = a1[e] + (kh ? acc[12 + e] : acc[4 + e]);
            }
        }
        float m1, sd1, m2, sd2;
        f32x4 p0 = *(const LAS f32x4*)(ps), p1 = *(const LAS f32x4*)(ps + 16);
        const float mean = ((p0[0] + p0[2]) + (p1[0] + p1[2])) * (1.0f / 512);
        const float msq = ((p0[1] + p0[3]) + (p1[1] + p1[3])) * (1.0f / 512);
        const float var = fmaxf(msq - mean * mean, 0.f);
        ln_consts(mean, var, m1, sd1);
        const float r1 = __builtin_amdgcn_rcpf(sd1), nb1 = -m1 * r1;
        ln_consts((mean - m1) * r1, var * r1 * r1, m2, sd2);
        const float alpha = r1 * __builtin_amdgcn_rcpf(sd2);
        const float mu = m1 + m2 * sd1;
        const float ga = GC1 * alpha, gb = GC3 * alpha * alpha * alpha;
        u32x4 o;
#pragma unroll
        for (int e = 0; e < 4; ++e) {
            float d0_ = __builtin_fmaf(-mu, csv[2 * e], fin[2 * e]), d1_ = __builtin_fmaf(-mu, csv[2 * e + 1], fin[2 * e + 1]);
            o[e] = pk_bf16(gelu_s(d0_, alpha, ga, gb), gelu_s(d1_, alpha, ga, gb));
        }
        __builtin_amdgcn_raw_buffer_store_b128(o, rs, WS_HG + (((tt * 2 + tq) * 32 + 4 * ct + 2 * nq + kh) * 64 + lane) * 16, 0, 16);
#pragma unroll
        for (int e = 0; e < 4; ++e)
            x1res[e] = pk_bf16(__builtin_fmaf(bf_lo(hres[e]), r1, nb1), __builtin_fmaf(bf_hi(hres[e]), r1, nb1));
    }
    WAIT_V(0);
    LBAR();
    if (tid == 0) __hip_atomic_store((GAS u32*)(ws + WS_F2) + (tt * 8 + ct) * 32, ep, __ATOMIC_RELAXED, __HIP_MEMORY_SCOPE_AGENT);
    W3LOAD(8, 24);

    {
        const LAS char* Wb = lds + A_W + kh * 8192 + (tr_off ^ (nq << 6));
#pragma unroll
        for (int i = 0; i < 16; ++i) wf[i] = tr_frag(Wb + ((i >> 2) * 8 + (i & 3)) * 2048);
    }
    if (wave < 7) CS3(0, 8);
    if (wave == 7) {
        const int tl = tt - 1 + (lane >> 3);
        const bool act = lane < 24 && tl >= 0 && tl < 32;
        const GAS u32* fp = (const GAS u32*)(ws + WS_F2) + ((act ? tl : tt) * 8 + (lane & 7)) * 32;
        _Pragma("unroll 1") for (int spin = 0; spin < SPIN_MAX; ++spin) {
            u32 f = __hip_atomic_load(fp, __ATOMIC_RELAXED, __HIP_MEMORY_SCOPE_AGENT);
            if (__all(f == ep)) break;
            __builtin_amdgcn_s_sleep(1);
        }
    }
    LBAR();
#pragma unroll
    for (int c = 0; c < 4; ++c) {
        const int ks = 8 * c + wave;
        const int sh = c == 0 ? -1 : (c == 1 ? 1 : (c == 2 ? -2 : 2));
#pragma unroll
        for (int i = 0; i < 2; ++i) {
            const int t = tt * 64 + i * 32 + r32 + sh;
            const bool ok = (unsigned)t < (unsigned)L;
            const char* src = ok ? ws + WS_HG + ((((t >> 5) * 32 + ks) * 64) + h32 * 32 + (t & 31)) * 16 : (const char*)g_zero16;
            dma_g(src, lbase + G_ACT + (i * 32 + ks) * 1024);
        }
    }
    {
        f32x16 acc = {};
#pragma unroll
        for (int c = 0; c < 4; ++c) {
            if (c == 0) WAIT_V(6); else if (c == 1) WAIT_V(4); else if (c == 2) WAIT_V(2); else WAIT_V(0);
            LBAR();
#pragma unroll
            for (int kk = 0; kk < 4; ++kk) {
                const int ks = 8 * c + 4 * kh + kk;
                u32x4 bb = *(const LAS u32x4*)(lds + G_ACT + (tq * 32 + ks) * 1024 + lane * 16);
                acc = mfma32(wf[4 * c + kk], bb, acc);
            }
        }
        W3LOAD(24, 32);
        {
            LAS char* p = lds + G_RED + wave * 2048 + lane * 16;
            if (kh == 0) { *(LAS f32x4*)(p) = f32x4{acc[8], acc[9], acc[10], acc[11]}; *(LAS f32x4*)(p + 1024) = f32x4{acc[12], acc[13], acc[14], acc[15]}; }
            else { *(LAS f32x4*)(p) = f32x4{acc[0], acc[1], acc[2], acc[3]}; *(LAS f32x4*)(p + 1024) = f32x4{acc[4], acc[5], acc[6], acc[7]}; }
        }
        LBAR();
        const LAS char* p = lds + G_RED + (wave ^ 4) * 2048 + lane * 16;
        f32x4 a0 = *(const LAS f32x4*)(p), a1 = *(const LAS f32x4*)(p + 1024);
        float fin[8];
#pragma unroll
        for (int e = 0; e < 4; ++e) {
            fin[e] = a0[e] + (kh ? acc[8 + e] : acc[e]);
            fin[4 + e] = a1[e] + (kh ? acc[12 + e] : acc[4 + e]);
        }
#pragma unroll
        for (int e = 0; e < 4; ++e)
            x1res[e] = pk_bf16v(unpk(x1res[e]) + unpk(pk_bf16(fin[2 * e], fin[2 * e + 1])));
        __builtin_amdgcn_raw_buffer_store_b128(x1res, rs, WS_X1 + (((tt * 2 + tq) * 32 + 4 * ct + 2 * nq + kh) * 64 + lane) * 16, 0, 16);
        {
            float sa = 0.f, qa = 0.f, sb = 0.f, qb = 0.f;
#pragma unroll
            for (int e = 0; e < 4; ++e) {
                const float lo = bf_lo(x1res[e]), hi = bf_hi(x1res[e]);
                sa += lo; sb += hi; qa = __builtin_fmaf(lo, lo, qa); qb = __builtin_fmaf(hi, hi, qb);
            }
            const float st = add_xor32(sa + sb), qt = add_xor32(qa + qb);
            if (h32 == 0)
                __builtin_amdgcn_raw_buffer_store_b64(u32x2{__float_as_uint(st), __float_as_uint(qt)}, rs,
                                                      WS_PST + ((tt * 2 + tq) * 32 + ct * 4 + nq * 2 + kh) * 256 + r32 * 8, 0, 16);
        }
        *(LAS u32x4*)(lds + G_SPARE + wave * 2048 + lane * 16) = x1res;
        *(LAS u32x4*)(lds + G_SPARE + wave * 2048 + 1024 + lane * 16) = hres;
    }
    WAIT_V(0);
    LBAR();
    if (tid == 0) __hip_atomic_store((GAS u32*)(ws + WS_F3) + (tt * 8 + ct) * 32, ep, __ATOMIC_RELAXED, __HIP_MEMORY_SCOPE_AGENT);
    const int kq = 2 * tq + kh;
    const u32 w4base = WS_WT4 + ((ct * 2 + nq) * 112 + kq * 28) * 1024;
    const u32 w4st = wave < 2 ? 65536 + wave * 8192 : (wave < 4 ? 81920 + wave * 8192 : 98304 + wave * 8192);
    dma_ws4(srd_ws, lane * 16, w4base + 20 * 1024, lbase + w4st);
    dma_ws4(srd_ws, lane * 16, w4base + 24 * 1024, lbase + w4st + 4096);

    const u32 w4off = w4base + lane * 16;
    u32x4 w4f[28];
    const GAS u32* f4p = (const GAS u32*)(ws + WS_F4) + (tt * 8 + (lane & 7)) * 32;
#define ACT3_DMA() do { \
        dma_ws4(srd_ws, lane * 16, WS_X1 + tt * 65536 + wave * 4096, lbase + G_ACT + wave * 4096); \
        dma_ws4(srd_ws, lane * 16, WS_X1 + tt * 65536 + 32768 + wave * 4096, lbase + G_ACT + 32768 + wave * 4096); } while (0)
#define POLL(ptr) do { _Pragma("unroll 1") for (int spin_ = 0; spin_ < SPIN_MAX; ++spin_) { \
        u32 f_ = __hip_atomic_load((ptr), __ATOMIC_RELAXED, __HIP_MEMORY_SCOPE_AGENT); \
        if (__all(f_ == ep)) break; \
        __builtin_amdgcn_s_sleep(1); } } while (0)
    if (wave == 7) {
        if (lane == 0) *(LAS u32*)(lds + G_CS + 2048) = 0u;
        POLL((const GAS u32*)(ws + WS_F3) + (tt * 8 + (lane & 7)) * 32);
        LBAR();
        ACT3_DMA();
        WAIT_V(4);
        LBAR();
        u32x2 pp[2][16];
#pragma unroll
        for (int i = 0; i < 2; ++i)
#pragma unroll
            for (int j = 0; j < 16; ++j)
                pp[i][j] = __builtin_amdgcn_raw_buffer_load_b64(rs, WS_PST + ((tt * 2 + i) * 32 + h32 * 16 + j) * 256 + r32 * 8, 0, 16);
#pragma unroll
        for (int i = 0; i < 2; ++i) {
            float sa = 0.f, qa = 0.f, sb = 0.f, qb = 0.f;
#pragma unroll
            for (int j = 0; j < 16; j += 2) {
                sa += __uint_as_float(pp[i][j][0]); qa += __uint_as_float(pp[i][j][1]);
                sb += __uint_as_float(pp[i][j + 1][0]); qb += __uint_as_float(pp[i][j + 1][1]);
            }
            float mean = add_xor32(sa + sb) * (1.0f / 512), msq = add_xor32(qa + qb) * (1.0f / 512);
            float m, sd;
            ln_consts(mean, fmaxf(msq - mean * mean, 0.f), m, sd);
            if (h32 == 0) *(LAS f32x2*)(lds + G_PS + (i * 32 + r32) * 8) = f32x2{__builtin_amdgcn_rcpf(sd), m};
        }
        LBAR();
#pragma unroll
        for (int j = 0; j < 20; ++j) { w4f[j] = __builtin_amdgcn_raw_buffer_load_b128(rs, w4off + j * 1024, 0, 16); __builtin_amdgcn_s_sleep(1); }
        FENCE();
#pragma unroll
        for (int j = 0; j < 8; ++j) w4f[20 + j] = *(const LAS u32x4*)(lds + w4st + j * 1024 + lane * 16);
        LBAR();
        POLL(f4p);
#pragma unroll
        for (int i = 0; i < 28; ++i) PIN4(w4f[i]);
        LBAR();
    } else {
        CS3(8, 32);
        {
            const float csum = add_xor32(c3a + c3b);
            if (h32 == 0) *(LAS float*)(lds + G_CS + (wave * 32 + r32) * 4) = csum;
        }
        LBAR();
        ACT3_DMA();
        const LAS char* Ab = lds + G_ACT + lane * 16;
        const u32 gdst = WS_G + ((tt * 2 * 112 + (ct * 7 + wave) * 2) * 64 + lane) * 16;
        f32x16 acc0 = {}, acc1 = {};
        u32x4 bq[4];
        WAIT_V(4);
        LBAR();
#pragma unroll
        for (int i = 0; i < 4; ++i) bq[i] = *(const LAS u32x4*)(Ab + i * 1024);
#pragma unroll
        for (int ks = 0; ks < 32; ++ks) {
            acc0 = mfma32(w3f[ks], bq[ks & 3], acc0);
            if (ks + 4 < 32) bq[ks & 3] = *(const LAS u32x4*)(Ab + (ks + 4) * 1024);
            if (ks % 3 == 0) w4f[ks / 3] = __builtin_amdgcn_raw_buffer_load_b128(rs, w4off + (ks / 3) * 1024, 0, 16);
            SCHED0();
        }
        WAIT_V(11);
        LBAR();
#pragma unroll
        for (int i = 0; i < 4; ++i) bq[i] = *(const LAS u32x4*)(Ab + (32 + i) * 1024);
        float cs[16];
#pragma unroll
        for (int i = 0; i < 4; ++i) {
            f32x4 c4 = *(const LAS f32x4*)(lds + G_CS + (wave * 32 + 8 * i + 4 * h32) * 4);
            cs[4 * i] = c4[0]; cs[4 * i + 1] = c4[1]; cs[4 * i + 2] = c4[2]; cs[4 * i + 3] = c4[3];
        }
        {
            const f32x2 st = *(const LAS f32x2*)(lds + G_PS + r32 * 8);
            const float ga = GC1 * st.x, gb = GC3 * st.x * st.x * st.x;
            float g[16];
            SCHED0();
#pragma unroll
            for (int k4 = 0; k4 < 32; k4 += 4) {
#pragma unroll
                for (int ks = k4; ks < k4 + 4; ++ks) {
                    acc1 = mfma32(w3f[ks], bq[ks & 3], acc1);
                    if (ks + 4 < 32) bq[ks & 3] = *(const LAS u32x4*)(Ab + (32 + ks + 4) * 1024);
                    if (ks < 16) g[ks] = gelu_s(__builtin_fmaf(-st.y, cs[ks], acc0[ks]), st.x, ga, gb);
                    if (ks >= 20 && ks < 29) w4f[11 + ks - 20] = __builtin_amdgcn_raw_buffer_load_b128(rs, w4off + (11 + ks - 20) * 1024, 0, 16);
                    if (ks >= 24) w4f[20 + ks - 24] = *(const LAS u32x4*)(lds + w4st + (ks - 24) * 1024 + lane * 16);
                }
                if (k4 == 8 || k4 == 16) {
                    const int o = k4 - 8;
                    u32x4 v = {pk_bf16(g[o], g[o + 1]), pk_bf16(g[o + 2], g[o + 3]), pk_bf16(g[o + 4], g[o + 5]), pk_bf16(g[o + 6], g[o + 7])};
                    __builtin_amdgcn_raw_buffer_store_b128(v, rs, gdst + (o >> 3) * 1024, 0, 16);
                }
#pragma unroll
                for (int q = 0; q < 4; ++q) {
                    __builtin_amdgcn_sched_group_barrier(0x008, 1, 0);
                    __builtin_amdgcn_sched_group_barrier(0x100, 1, 0);
                    __builtin_amdgcn_sched_group_barrier(0x002, 10, 0);
                    __builtin_amdgcn_sched_group_barrier(0x010, 1, 0);
                }
                SCHED0();
            }
        }
        WAIT_V(9);
        LBAR();
        if (tid == 0) __hip_atomic_store((GAS u32*)(ws + WS_F4) + (tt * 8 + ct) * 32, ep, __ATOMIC_RELAXED, __HIP_MEMORY_SCOPE_AGENT);
        {
            const f32x2 st = *(const LAS f32x2*)(lds + G_PS + (32 + r32) * 8);
            const float ga = GC1 * st.x, gb = GC3 * st.x * st.x * st.x;
#pragma unroll
            for (int sI = 0; sI < 2; ++sI) {
                u32x4 v;
#pragma unroll
                for (int e = 0; e < 4; ++e) {
                    const int j = 8 * sI + 2 * e;
                    v[e] = pk_bf16(gelu_s(__builtin_fmaf(-st.y, cs[j], acc1[j]), st.x, ga, gb),
                                   gelu_s(__builtin_fmaf(-st.y, cs[j + 1], acc1[j + 1]), st.x, ga, gb));
                }
                __builtin_amdgcn_raw_buffer_store_b128(v, rs, gdst + 112 * 1024 + sI * 1024, 0, 16);
            }
        }
        WAIT_V(0);
        if (lane == 0) {
            const u32 old = __hip_atomic_fetch_add((LAS u32*)(lds + G_CS + 2048), 1u, __ATOMIC_RELAXED, __HIP_MEMORY_SCOPE_WORKGROUP);
            if (old == 6) __hip_atomic_store((GAS u32*)(ws + WS_F5) + (tt * 8 + ct) * 32, ep, __ATOMIC_RELAXED, __HIP_MEMORY_SCOPE_AGENT);
        }
#pragma unroll
        for (int i = 0; i < 28; ++i) PIN4(w4f[i]);
        LBAR();
    }
    {
        const u32 gsrc = WS_G + (tt * 2 * 112 + wave * 14) * 1024;
        const u32 ldst = lbase + wave * 14336;
#define FILL_A(t) do { dma_ws4(srd_ws, lane * 16, gsrc + (t) * 114688, ldst); dma_ws4(srd_ws, lane * 16, gsrc + (t) * 114688 + 4096, ldst + 4096); } while (0)
#define FILL_B(t, dst) do { dma_ws4(srd_ws, lane * 16, gsrc + (t) * 114688 + 8192, (dst)); dma_ws2(srd_ws, lane * 16, gsrc + (t) * 114688 + 12288, (dst) + 4096); } while (0)
        FILL_A(0); FILL_B(0, ldst + 8192);
        x1res = *(const LAS u32x4*)(lds + G_SPARE + wave * 2048 + lane * 16);
        hres = *(const LAS u32x4*)(lds + G_SPARE + wave * 2048 + 1024 + lane * 16);
        const LAS char* Gb = lds + kq * 28672 + lane * 16;
        const LAS char* Gs = lds + G_SPARE + kq * 12288 + lane * 16;
        f32x16 acc0 = {}, acc1 = {};
        u32x4 bq[4];
#define KA(i) ((i) < 8 ? (i) : (i) + 6)
#define KB(i) ((i) < 6 ? (i) + 8 : (i) + 16)
#define KS(i) (i)
#define PART(ACC, BASE, KF, KW, N) do { \
        _Pragma("unroll") for (int i_ = 0; i_ < 4; ++i_) bq[i_] = *(const LAS u32x4*)(BASE + KF(i_) * 1024); \
        _Pragma("unroll") for (int i_ = 0; i_ < N; ++i_) { \
            ACC = mfma32(w4f[KW(i_)], bq[i_ & 3], ACC); \
            if (i_ + 4 < N) bq[i_ & 3] = *(const LAS u32x4*)(BASE + KF(i_ + 4) * 1024); \
            SCHED0(); } } while (0)
        if (wave == 7) POLL(f4p + 8192);
        WAIT_V(6);
        LBAR();
        {
#pragma unroll
            for (int i_ = 0; i_ < 4; ++i_) bq[i_] = *(const LAS u32x4*)(Gb + KA(i_) * 1024);
#pragma unroll
            for (int i_ = 0; i_ < 16; ++i_) {
                if (i_ % 4 == 1 && i_ < 12) dma_ws2(srd_ws, lane * 16, gsrc + 114688 + 8192 + (i_ / 4) * 2048, lbase + G_SPARE + wave * 6144 + (i_ / 4) * 2048);
                acc0 = mfma32(w4f[KA(i_)], bq[i_ & 3], acc0);
                if (i_ + 4 < 16) bq[i_ & 3] = *(const LAS u32x4*)(Gb + KA(i_ + 4) * 1024);
                SCHED0();
            }
        }
        WAIT_V(6);
        LBAR();
        {
#pragma unroll
            for (int i_ = 0; i_ < 4; ++i_) bq[i_] = *(const LAS u32x4*)(Gb + KB(i_) * 1024);
#pragma unroll
            for (int i_ = 0; i_ < 12; ++i_) {
                if (i_ < 4) dma_ws2(srd_ws, lane * 16, gsrc + 114688 + i_ * 2048, ldst + i_ * 2048);
                acc0 = mfma32(w4f[KB(i_)], bq[i_ & 3], acc0);
                if (i_ + 4 < 12) bq[i_ & 3] = *(const LAS u32x4*)(Gb + KB(i_ + 4) * 1024);
                SCHED0();
            }
        }
        WAIT_V(8);
        LBAR();
        PART(acc1, Gs, KS, KB, 12);
        WAIT_V(0);
        LBAR();
        PART(acc1, Gb, KA, KA, 16);
#pragma unroll
        for (int p = 0; p < 4; ++p) {
            if (p != kq) {
                const int slot = kq > p ? kq - 1 : kq;
                LAS char* dst = lds + G_SPARE + ((nq * 4 + p) * 3 + slot) * 2048 + lane * 16;
                const int o = 8 * (p & 1);
                if (p < 2) { *(LAS f32x4*)(dst) = f32x4{acc0[o], acc0[o + 1], acc0[o + 2], acc0[o + 3]};
                             *(LAS f32x4*)(dst + 1024) = f32x4{acc0[o + 4], acc0[o + 5], acc0[o + 6], acc0[o + 7]}; }
                else { *(LAS f32x4*)(dst) = f32x4{acc1[o], acc1[o + 1], acc1[o + 2], acc1[o + 3]};
                       *(LAS f32x4*)(dst + 1024) = f32x4{acc1[o + 4], acc1[o + 5], acc1[o + 6], acc1[o + 7]}; }
            }
        }
        float fin[8];
#pragma unroll
        for (int e = 0; e < 8; ++e) {
            float lo = tq ? acc1[e] : acc0[e], hi = tq ? acc1[8 + e] : acc0[8 + e];
            fin[e] = kh ? hi : lo;
        }
        PIN4(hres); PIN4(x1res);
        LBAR();
#pragma unroll
        for (int sl = 0; sl < 3; ++sl) {
            const LAS char* src = lds + G_SPARE + ((nq * 4 + kq) * 3 + sl) * 2048 + lane * 16;
            f32x4 a0 = *(const LAS f32x4*)(src), a1 = *(const LAS f32x4*)(src + 1024);
#pragma unroll
            for (int e = 0; e < 4; ++e) { fin[e] += a0[e]; fin[4 + e] += a1[e]; }
        }
        u32 o[4];
#pragma unroll
        for (int e = 0; e < 4; ++e) {
            f32x2 h2 = unpk(pk_bf16(fin[2 * e], fin[2 * e + 1]));
            f32x2 t1 = unpk(pk_bf16v(unpk(x1res[e]) + h2));
            o[e] = pk_bf16v(unpk(hres[e]) + t1);
        }
        int tz = threadIdx.x;
        asm volatile("" : "+v"(tz));
        u16* orow = out + (size_t)(tt * 64 + tq * 32 + (tz & 31)) * D + ct * 64 + nq * 32 + kh * 16 + 4 * ((tz >> 5) & 1);
        *(u32x2*)(orow) = u32x2{o[0], o[1]};
        *(u32x2*)(orow + 8) = u32x2{o[2], o[3]};
    }
}

extern "C" void kernel_launch(void* const* d_in, const int* in_sizes, int n_in, void* d_out, int out_size,
                              void* d_ws, size_t ws_size, hipStream_t stream) {
    (void)hipFuncSetAttribute((const void*)k_mega, hipFuncAttributeMaxDynamicSharedMemorySize, LDS_BYTES);
    k_mega<<<256, 512, LDS_BYTES, stream>>>((const u16*)d_in[0], (const u16*)d_in[1], (const u16*)d_in[2],
                                            (const u16*)d_in[3], (const u16*)d_in[4], (const u16*)d_in[5],
                                            (const u16*)d_in[6], (const u16*)d_in[7], (const u16*)d_in[8],
                                            (const u16*)d_in[10], (const u16*)d_in[11], (const u16*)d_in[12],
                                            (const u16*)d_in[13], (u16*)d_out, (char*)d_ws);
}
```
